```python
import torch

def _softmax(x, axis=-1):
    return torch.softmax(x, dim=axis)
def _gelu(x):
    return torch.nn.functional.gelu(x, approximate="tanh")
def _silu(x):
    return torch.nn.functional.silu(x)
def _sigmoid(x):
    return torch.sigmoid(x)
def _layernorm(x, axis=-1, eps=1e-5):
    m = x.mean(dim=axis, keepdim=True)
    v = x.var(dim=axis, keepdim=True, unbiased=False)
    return (x - m) / torch.sqrt(v + eps)
def _scatter_add(tgt, idx, src, axis=0):
    return tgt.clone().index_add_(axis, idx, src)
def _segment_sum(src, seg, n):
    out = torch.zeros((n,) + src.shape[1:], dtype=src.dtype)
    return out.index_add_(0, seg, src)
def _pad(x, pw):
    flat = tuple(v for pair in reversed(pw) for v in pair)
    return torch.nn.functional.pad(x, flat)

kernel_name = 'recurrent_scan__linear_kernel_hedgehog__glu_gated_swiglu__prompt'

def setup_inputs():

    _w = (lambda a: (lambda t: t.to(torch.bfloat16) if t.is_floating_point() else t)(torch.as_tensor(a)))
    def _A_setup():
        B, S, D, H = 1, 2048, 512, 32
        Dh = D // H
        return {
            "x": _w((lambda *s: torch.randn(*s, dtype=torch.bfloat16) * 0.05)(B, S, D)),
            "wqkv": _w((lambda *s: torch.randn(*s, dtype=torch.bfloat16) * 0.8)(D, 3 * D)),
            "w_phi": _w((lambda *s: torch.randn(*s, dtype=torch.bfloat16) * 0.8)(H, Dh, Dh) * (Dh ** -0.5)),
            "wo": _w((lambda *s: torch.randn(*s, dtype=torch.bfloat16) * 0.8)(D, D)),
        }
    _A_in = _A_setup()

    def _B_setup():
        B, S, D, F = 1, 2048, 512, 1376
        return {
            "x": _w((lambda *s: torch.randn(*s, dtype=torch.bfloat16) * 0.05)(B, S, D)),
            "w1": _w((lambda *s: torch.randn(*s, dtype=torch.bfloat16) * 0.2)(D, F) * (2.0 / D) ** 0.5),
            "w3": _w((lambda *s: torch.randn(*s, dtype=torch.bfloat16) * 0.2)(D, F) * (2.0 / D) ** 0.5),
            "w2": _w((lambda *s: torch.randn(*s, dtype=torch.bfloat16) * 0.2)(F, D) * (2.0 / F) ** 0.5),
        }
    _B_in = _B_setup()

    return {
        "A_x": _A_in["x"],
        "A_wqkv": _A_in["wqkv"],
        "A_w_phi": _A_in["w_phi"],
        "A_wo": _A_in["wo"],
        "B_x": _B_in["x"],
        "B_w1": _B_in["w1"],
        "B_w3": _B_in["w3"],
        "B_w2": _B_in["w2"],
    }

def reference(A_x, A_wqkv, A_w_phi, A_wo, B_x, B_w1, B_w3, B_w2):
    def _A_ref(x, wqkv, w_phi, wo):
        B, S, D = x.shape
        H = 32; Dh = D // H
        qkv = (lambda x, *d: x.permute(*d))((x @ wqkv).reshape(B, S, 3, H, Dh), 2, 0, 3, 1, 4)
        q, k, v = qkv[0], qkv[1], qkv[2]
        pq = torch.einsum("bhsd,hdf->bhsf", q, w_phi)
        pk = torch.einsum("bhsd,hdf->bhsf", k, w_phi)
        phi_q = torch.concatenate([_softmax(pq, axis=-1), _softmax(-pq, axis=-1)], axis=-1)
        phi_k = torch.concatenate([_softmax(pk, axis=-1), _softmax(-pk, axis=-1)], axis=-1)
        kv = torch.einsum("bhsf,bhsd->bhfd", phi_k, v)
        z  = torch.einsum("bhsf->bhf", phi_k)
        num = torch.einsum("bhqf,bhfd->bhqd", phi_q, kv)
        den = torch.einsum("bhqf,bhf->bhq", phi_q, z)[..., None] + 1e-6
        o = num / den
        return (lambda x, *d: x.permute(*d))(o, 0, 2, 1, 3).reshape(B, S, D) @ wo

    def _B_ref(x, w1, w3, w2):
        gate = torch.einsum("bsd,df->bsf", x, w1)
        up = torch.einsum("bsd,df->bsf", x, w3)
        h = _silu(gate) * up
        out = torch.einsum("bsf,fd->bsd", h, w2)
        return out

    x = A_x
    xn = _layernorm(x, axis=-1)
    s = _A_ref(xn, A_wqkv, A_w_phi, A_wo)
    g = _silu(_B_ref(xn, B_w1, B_w3, B_w2))
    return x + s * g
```

```cpp
#include <hip/hip_runtime.h>
#include <hip/hip_bf16.h>
#include <stdint.h>

typedef __bf16 bf16x2 __attribute__((ext_vector_type(2)));
typedef __bf16 bf16x8 __attribute__((ext_vector_type(8)));
typedef float f32x2 __attribute__((ext_vector_type(2)));
typedef float f32x4 __attribute__((ext_vector_type(4)));
typedef float f32x16 __attribute__((ext_vector_type(16)));
typedef short s16x4 __attribute__((ext_vector_type(4)));
typedef unsigned u32x2 __attribute__((ext_vector_type(2)));
typedef unsigned u32x4 __attribute__((ext_vector_type(4)));
typedef unsigned short u16;

#define LAS __attribute__((address_space(3)))
#define GAS __attribute__((address_space(1)))
#define DEVI __device__ __forceinline__

constexpr int S_ = 2048, D_ = 512, F_ = 1376;
constexpr int NU = 86;

constexpr size_t WS_H    = 0;
constexpr size_t WS_PQ   = WS_H + (size_t)64 * NU * 1024;
constexpr size_t WS_W2   = WS_PQ + (size_t)64 * 32 * 2 * 1024;
constexpr size_t WS_WO   = WS_W2 + (size_t)NU * 16 * 1024;
constexpr size_t WS_KVP  = WS_WO + (size_t)32 * 16 * 1024;
constexpr size_t WS_KVR  = WS_KVP + (size_t)16 * 32 * 544 * 4;
constexpr size_t WS_FLAG = WS_KVR + (size_t)32 * 544 * 2;
static_assert(WS_FLAG % 256 == 0, "align");

#define WAIT_V(n) asm volatile("s_waitcnt vmcnt(" #n ")" ::: "memory")
#define WAIT_L0() asm volatile("s_waitcnt lgkmcnt(0)" ::: "memory")
#define BAR() __builtin_amdgcn_s_barrier()

DEVI float rbf(float x) {
  f32x2 t = {0.f, x};
  return __builtin_bit_cast(float, __builtin_convertvector(t, bf16x2));
}
DEVI unsigned pk2(float a, float b) {
  f32x2 t = {a, b};
  return __builtin_bit_cast(unsigned, __builtin_convertvector(t, bf16x2));
}
DEVI float lo16(unsigned w) { return __builtin_bit_cast(float, w << 16); }
DEVI float hi16(unsigned w) { return __builtin_bit_cast(float, w & 0xffff0000u); }
DEVI f32x16 mfma32(u32x4 a, u32x4 b, f32x16 c) {
  return __builtin_amdgcn_mfma_f32_32x32x16_bf16(__builtin_bit_cast(bf16x8, a), __builtin_bit_cast(bf16x8, b), c, 0, 0, 0);
}
DEVI u32x2 trread(const LAS char* p) {
  return __builtin_bit_cast(u32x2, __builtin_amdgcn_ds_read_tr16_b64_v4i16((LAS s16x4*)p));
}
DEVI u32x4 trfrag(const LAS char* p, int step) {
  u32x2 a = trread(p), b = trread(p + step);
  u32x4 r = {a[0], a[1], b[0], b[1]};
  return r;
}
DEVI f32x2 xhalf(float v) {
  const unsigned a = __float_as_uint(v);
  auto s = __builtin_amdgcn_permlane32_swap(a, a, false, false);
  const unsigned s0 = s[0], s1 = s[1];
  f32x2 r = {__uint_as_float(s0), __uint_as_float(s1)};
  return r;
}
DEVI float xhalf_max(float v) { f32x2 r = xhalf(v); return fmaxf(r[0], r[1]); }
DEVI float xhalf_sum(float v) { f32x2 r = xhalf(v); return r[0] + r[1]; }
template <int CTRL> DEVI float dpp_f(float v) {
  return __builtin_bit_cast(float, __builtin_amdgcn_update_dpp(0, __builtin_bit_cast(int, v), CTRL, 0xf, 0xf, false));
}
DEVI float sum8(float v) {
  v += dpp_f<0xB1>(v);
  v += dpp_f<0x4E>(v);
  v += dpp_f<0x141>(v);
  return v;
}
DEVI float silu_f(float g) { return g * __builtin_amdgcn_rcpf(1.0f + __builtin_amdgcn_exp2f(-1.4426950408889634f * g)); }

DEVI void feature_map(const f32x16& acc, u32x4 wp, bool upper, u32x4 (&out)[2][2]) {
  u32x4 x0, x1;
#pragma unroll
  for (int i = 0; i < 4; ++i) {
    x0[i] = pk2(acc[2 * i], acc[2 * i + 1]);
    x1[i] = pk2(acc[8 + 2 * i], acc[8 + 2 * i + 1]);
  }
  u32x4 z = {0u, 0u, 0u, 0u};
  u32x4 a0 = upper ? z : wp, a1 = upper ? wp : z;
  f32x16 p = {};
  p = mfma32(a0, x0, p);
  p = mfma32(a1, x1, p);
  const float L = 1.4426950408889634f;
#pragma unroll
  for (int s = 0; s < 2; ++s) {
    float v[8];
#pragma unroll
    for (int j = 0; j < 8; ++j) v[j] = rbf(p[8 * s + j]);
    float mx = fmaxf(fmaxf(fmaxf(v[0], v[1]), fmaxf(v[2], v[3])), fmaxf(fmaxf(v[4], v[5]), fmaxf(v[6], v[7])));
    float mn = fminf(fminf(fminf(v[0], v[1]), fminf(v[2], v[3])), fminf(fminf(v[4], v[5]), fminf(v[6], v[7])));
    mx = xhalf_max(mx);
    mn = -xhalf_max(-mn);
    float ep[8], en[8], sp = 0.f, sn = 0.f;
#pragma unroll
    for (int j = 0; j < 8; ++j) {
      ep[j] = __builtin_amdgcn_exp2f((v[j] - mx) * L);
      en[j] = __builtin_amdgcn_exp2f((mn - v[j]) * L);
      sp += ep[j];
      sn += en[j];
    }
    sp = __builtin_amdgcn_rcpf(xhalf_sum(sp));
    sn = __builtin_amdgcn_rcpf(xhalf_sum(sn));
#pragma unroll
    for (int i = 0; i < 4; ++i) {
      out[s][0][i] = pk2(ep[2 * i] * sp, ep[2 * i + 1] * sp);
      out[s][1][i] = pk2(en[2 * i] * sn, en[2 * i + 1] * sn);
    }
  }
}

DEVI u32x4 ffn_epi(const f32x16& acc) {
  float h[8];
#pragma unroll
  for (int j = 0; j < 8; ++j) {
    float g = rbf(acc[j]), u = rbf(acc[8 + j]);
    h[j] = rbf(silu_f(g)) * u;
  }
  u32x4 r;
#pragma unroll
  for (int i = 0; i < 4; ++i) r[i] = pk2(h[2 * i], h[2 * i + 1]);
  return r;
}

constexpr int L_S = 131072;
constexpr int L_PHI = L_S;
constexpr int L_V = L_S + 16384;
constexpr int L_ONES = L_S + 24576;

__global__ __launch_bounds__(512, 2) void k1_kernel(const u16* __restrict__ x, const u16* __restrict__ wqkv,
                                                    const u16* __restrict__ wphi, const u16* __restrict__ wo,
                                                    const u16* __restrict__ w1, const u16* __restrict__ w3,
                                                    const u16* __restrict__ w2, char* __restrict__ ws) {
  __shared__ __attribute__((aligned(16))) char lds_raw[163840];
  LAS char* lds = (LAS char*)lds_raw;
  const int b = blockIdx.x, tid = threadIdx.x, l = tid & 63;
  const int w = __builtin_amdgcn_readfirstlane(tid >> 6);
  const int xcd = b & 7, idx = b >> 3;
  const int rb = (xcd & 1) * 8 + (idx & 7);
  const int c = (xcd >> 1) * 4 + (idx >> 3);
  const int nfb = c < 11 ? 3 : 2;
  const int fb0 = c < 11 ? 3 * c : 33 + 2 * (c - 11);
  const int rg = w & 3, hw = w >> 2;
  const int hh = l >> 5, l31 = l & 31;
  const int tg = rb * 4 + rg;

  if (tid == 0) ((unsigned*)(ws + WS_FLAG))[b] = 0u;

  u32x4 xr[16];
  {
    const char* xp = (const char*)x + (size_t)(rb * 128 + 16 * w + (l >> 3)) * 1024 + 16 * (l & 7);
#pragma unroll
    for (int i = 0; i < 16; ++i) xr[i] = *(const u32x4*)(xp + (i >> 3) * 8192 + (i & 7) * 128);
  }
  u32x4 wp;
  {
    const u16* p = wphi + (2 * c + (l31 >> 4)) * 256 + (l31 & 15);
    unsigned t[8];
#pragma unroll
    for (int j = 0; j < 8; ++j) t[j] = p[((j & 3) + 8 * (j >> 2) + 4 * hh) * 16];
#pragma unroll
    for (int i = 0; i < 4; ++i) wp[i] = t[2 * i] | (t[2 * i + 1] << 16);
  }
  {
    const int fi = b * 8 + w;
    if (fi < 1888) {
      unsigned t[8];
      char* dst;
      if (fi < 1376) {
        const u16* p = w2 + (size_t)((fi >> 4) * 16 + 8 * hh) * 512 + (fi & 15) * 32 + l31;
#pragma unroll
        for (int j = 0; j < 8; ++j) t[j] = p[j * 512];
        dst = ws + WS_W2 + (size_t)fi * 1024;
      } else {
        const int g = fi - 1376;
        const u16* p = wo + (size_t)((g >> 4) * 16 + 4 * hh) * 512 + (g & 15) * 32 + l31;
#pragma unroll
        for (int j = 0; j < 8; ++j) t[j] = p[((j & 3) + 8 * (j >> 2)) * 512];
        dst = ws + WS_WO + (size_t)g * 1024;
      }
      u32x4 r;
#pragma unroll
      for (int i = 0; i < 4; ++i) r[i] = t[2 * i] | (t[2 * i + 1] << 16);
      *(u32x4*)(dst + 16 * l) = r;
    }
  }

  const unsigned lds0 = (unsigned)(uintptr_t)lds;
  const unsigned vo_qkv = (unsigned)((l >> 2) * 3072 + 16 * (l & 3));
  const unsigned vo_f = (unsigned)((l >> 2) * 2752 + 16 * ((l & 3) ^ (2 * hw)));
  const char* sb_kv = (const char*)wqkv + (size_t)(64 * rg) * 3072 + (512 * (1 + hw) + 32 * c) * 2;
  const char* sb_q = (const char*)wqkv + (size_t)(64 * w) * 3072 + (32 * c) * 2;
  const char* sb_f = (const char*)(hw ? w3 : w1) + (size_t)(64 * rg) * 2752 + (fb0 * 32) * 2;
  auto dma = [&](unsigned vo, const char* sb, unsigned dst) {
    asm volatile("s_mov_b32 m0, %2\n\ts_nop 0\n\tglobal_load_lds_dwordx4 %0, %1" ::"v"(vo), "s"(sb), "s"(dst) : "memory");
  };
  auto dma_kv = [&](int slot, int khalf) {
#pragma unroll
    for (int i = 0; i < 4; ++i) dma(vo_qkv, sb_kv + (size_t)(khalf * 256 + 16 * i) * 3072, lds0 + slot + 4096 * w + 1024 * i);
  };
  auto dma_q = [&](int slot) {
#pragma unroll
    for (int i = 0; i < 4; ++i) dma(vo_qkv, sb_q + (size_t)(16 * i) * 3072, lds0 + slot + 4096 * w + 1024 * i);
  };
  auto dma_f = [&](int slot, int blk, int khalf) {
#pragma unroll
    for (int i = 0; i < 4; ++i) dma(vo_f, sb_f + (size_t)(khalf * 256 + 16 * i) * 2752 + blk * 64, lds0 + slot + 4096 * w + 1024 * i);
  };

#pragma unroll
  for (int g8 = 0; g8 < 2; ++g8) {
    f32x2 xf[32];
    f32x2 s1 = {0.f, 0.f}, s2 = {0.f, 0.f};
#pragma unroll
    for (int i = 0; i < 8; ++i)
#pragma unroll
      for (int e = 0; e < 4; ++e) {
        unsigned wd = xr[g8 * 8 + i][e];
        f32x2 t = {lo16(wd), hi16(wd)};
        xf[i * 4 + e] = t;
        s1 += t;
        s2 += t * t;
      }
    float sum = sum8(s1[0] + s1[1]), sq = sum8(s2[0] + s2[1]);
    float mean = sum * (1.0f / 512.0f);
    float var = fmaxf(sq * (1.0f / 512.0f) - mean * mean, 0.f);
    float mb = rbf(mean);
    float sd = rbf(sqrtf(rbf(rbf(var) + 1e-5f)));
    float ri = 1.0f / sd;
    if (g8 == 1) {
      asm volatile("" : "+v"(wp));
      dma_kv(L_S, 0);
    }
    const int row = 16 * w + 8 * g8 + (l >> 3);
    LAS char* dst = lds + row * 1024;
#pragma unroll
    for (int i = 0; i < 8; ++i) {
      u32x4 o;
#pragma unroll
      for (int e = 0; e < 4; ++e) {
        f32x2 t = xf[i * 4 + e] - mb;
        f32x2 r = {rbf(t[0]), rbf(t[1])};
        r *= ri;
        o[e] = pk2(r[0], r[1]);
      }
      *(LAS u32x4*)(dst + 16 * ((8 * i + (l & 7)) ^ (row & 15))) = o;
    }
  }
  WAIT_L0();
  BAR();
  u32x4 xa[32];
  {
    const int row = 32 * rg + l31;
    const LAS char* src = lds + row * 1024;
#pragma unroll
    for (int t = 0; t < 32; ++t) xa[t] = *(const LAS u32x4*)(src + 16 * ((2 * t + hh) ^ (row & 15)));
  }
  WAIT_L0();
  WAIT_V(0);
  BAR();
  dma_kv(0, 1);
  dma_q(32768);
  dma_f(65536, 0, 0);
  dma_f(98304, 0, 1);

  const int g4 = l >> 4, q4 = (l >> 2) & 3, p4 = l & 3;
  const int krow = 8 * (g4 >> 1) + q4;
  const int off_q = krow * 64 + 32 * (g4 & 1) + 8 * p4;
  const int off_kv = off_q + 16384 * hw;
  const int off_f = 16384 * (g4 & 1) + krow * 64 + 32 * (hw ^ (g4 & 1)) + 16 * (p4 & 1) + 8 * (p4 >> 1);

  f32x16 acc = {};
#pragma unroll
  for (int t = 0; t < 16; ++t) acc = mfma32(trfrag(lds + L_S + off_kv + 1024 * t, 256), xa[t], acc);
  WAIT_L0();
  WAIT_V(12);
  BAR();
#pragma unroll
  for (int t = 0; t < 16; ++t) acc = mfma32(trfrag(lds + off_kv + 1024 * t, 256), xa[16 + t], acc);
  WAIT_L0();
  WAIT_V(8);
  BAR();
  dma_f(0, 1, 0);
  f32x16 accq = {};
  {
    const int tok = 32 * rg + l31;
    if (hw == 0) {
      u32x4 ph[2][2];
      feature_map(acc, wp, l31 >= 16, ph);
#pragma unroll
      for (int s = 0; s < 2; ++s)
#pragma unroll
        for (int ks = 0; ks < 2; ++ks)
          *(LAS u32x4*)(lds + L_PHI + s * 8192 + tok * 64 + (ks * 2 + hh) * 16) = ph[s][ks];
      if (tid < 8) {
        u32x4 ones = {0x3f803f80u, 0x3f803f80u, 0x3f803f80u, 0x3f803f80u};
        *(LAS u32x4*)(lds + L_ONES + 16 * tid) = ones;
      }
    } else {
#pragma unroll
      for (int a = 0; a < 4; ++a) {
        u32x2 o = {pk2(acc[4 * a], acc[4 * a + 1]), pk2(acc[4 * a + 2], acc[4 * a + 3])};
        *(LAS u32x2*)(lds + L_V + tok * 64 + (8 * a + 4 * hh) * 2) = o;
      }
#pragma unroll
      for (int t = 0; t < 32; ++t) accq = mfma32(trfrag(lds + 32768 + off_q + 1024 * t, 256), xa[t], accq);
    }
  }
  WAIT_L0();
  WAIT_V(8);
  BAR();
  dma_f(32768, 1, 1);
  acc = (f32x16){};
  if (hw == 0) {
    if (w < 2) {
      f32x16 kv = {};
      const LAS char* pa = lds + L_PHI + w * 8192 + krow * 64 + 32 * (g4 & 1) + 8 * p4;
      const LAS char* pb = (g4 & 1) ? (lds + L_ONES + q4 * 32 + 8 * p4) : (lds + L_V + krow * 64 + 32 * w + 8 * p4);
      const int bstep = (g4 & 1) ? 0 : 1024, b4 = (g4 & 1) ? 0 : 256;
#pragma unroll
      for (int t = 0; t < 8; ++t) kv = mfma32(trfrag(pa + 1024 * t, 256), trfrag(pb + bstep * t, b4), kv);
      float* dst = (float*)(ws + WS_KVP) + (size_t)(rb * 32 + 2 * c + w) * 544;
      if (l31 <= 16) {
#pragma unroll
        for (int a = 0; a < 4; ++a) {
          f32x4 o = {kv[4 * a], kv[4 * a + 1], kv[4 * a + 2], kv[4 * a + 3]};
          const int e = l31 < 16 ? (a * 16 + l31) * 8 + 4 * hh : 512 + 8 * a + 4 * hh;
          *(f32x4*)(dst + e) = o;
        }
      }
    }
#pragma unroll
    for (int t = 0; t < 16; ++t) acc = mfma32(trfrag(lds + 65536 + off_f + 1024 * t, 256), xa[t], acc);
  } else {
#pragma unroll
    for (int t = 0; t < 16; ++t) acc = mfma32(trfrag(lds + 65536 + off_f + 1024 * t, 256), xa[t], acc);
    u32x4 ph[2][2];
    feature_map(accq, wp, l31 >= 16, ph);
    char* dst = ws + WS_PQ + (size_t)(tg * 32 + 2 * c) * 2048 + 16 * l;
#pragma unroll
    for (int s = 0; s < 2; ++s)
#pragma unroll
      for (int ks = 0; ks < 2; ++ks) *(u32x4*)(dst + (s * 2 + ks) * 1024) = ph[s][ks];
  }
  WAIT_L0();
  WAIT_V(8);
  BAR();
  char* hdst = ws + WS_H + (size_t)(tg * NU + 2 * fb0 + hw) * 1024 + 16 * l;
  if (nfb == 3) dma_f(65536, 2, 0);
#pragma unroll
  for (int t = 0; t < 16; ++t) acc = mfma32(trfrag(lds + 98304 + off_f + 1024 * t, 256), xa[16 + t], acc);
  WAIT_L0();
  if (nfb == 3) { WAIT_V(8); } else { WAIT_V(4); }
  BAR();
  if (nfb == 3) dma_f(98304, 2, 1);
  {
    f32x16 a2 = {};
#pragma unroll
    for (int t = 0; t < 16; ++t) a2 = mfma32(trfrag(lds + 0 + off_f + 1024 * t, 256), xa[t], a2);
    *(u32x4*)(hdst) = ffn_epi(acc);
    acc = a2;
  }
  WAIT_L0();
  if (nfb == 3) { WAIT_V(8); } else { WAIT_V(0); }
  BAR();
#pragma unroll
  for (int t = 0; t < 16; ++t) acc = mfma32(trfrag(lds + 32768 + off_f + 1024 * t, 256), xa[16 + t], acc);
  if (nfb == 3) {
    WAIT_L0();
    WAIT_V(4);
    BAR();
    {
      f32x16 a2 = {};
#pragma unroll
      for (int t = 0; t < 16; ++t) a2 = mfma32(trfrag(lds + 65536 + off_f + 1024 * t, 256), xa[t], a2);
      *(u32x4*)(hdst + 2048) = ffn_epi(acc);
      acc = a2;
    }
    WAIT_L0();
    WAIT_V(0);
    BAR();
#pragma unroll
    for (int t = 0; t < 16; ++t) acc = mfma32(trfrag(lds + 98304 + off_f + 1024 * t, 256), xa[16 + t], acc);
    *(u32x4*)(hdst + 4096) = ffn_epi(acc);
  } else {
    *(u32x4*)(hdst + 2048) = ffn_epi(acc);
  }
}

constexpr int RED_PITCH = 272;

__global__ __launch_bounds__(512, 2) void k2_kernel(const u16* __restrict__ x, char* ws, u16* __restrict__ out) {
  __shared__ __attribute__((aligned(16))) char lds_raw[8 * 64 * RED_PITCH];
  LAS char* lds = (LAS char*)lds_raw;
  const int b = blockIdx.x, tid = threadIdx.x, l = tid & 63;
  const int w = __builtin_amdgcn_readfirstlane(tid >> 6);
  const int xcd = b & 7, idx = b >> 3;
  const int rb = xcd * 4 + (idx & 3);
  const int ct = idx >> 2;
  const int hh = l >> 5, l31 = l & 31;
  const int tg0 = 2 * rb, nt0 = 2 * ct;

  const size_t xoff = (size_t)(rb * 64 + (tid >> 3)) * 512 + ct * 64 + 8 * (tid & 7);
  const u32x4 xv = *(const u32x4*)(x + xoff);

  f32x16 acc[2][2] = {};
  if (w < 5) {
    const int u0 = w == 0 ? 0 : 1 + 17 * w;
    const char* pa = ws + WS_H + (size_t)(tg0 * NU + u0) * 1024 + 16 * l;
    const char* pb = ws + WS_W2 + (size_t)(u0 * 16 + nt0) * 1024 + 16 * l;
    constexpr int PD = 6;
    u32x4 fa[PD][2], fb[PD][2];
#pragma unroll
    for (int s = 0; s < PD; ++s) {
      fa[s][0] = *(const u32x4*)(pa + s * 1024);
      fa[s][1] = *(const u32x4*)(pa + s * 1024 + NU * 1024);
      fb[s][0] = *(const u32x4*)(pb + s * 16384);
      fb[s][1] = *(const u32x4*)(pb + s * 16384 + 1024);
    }
#pragma unroll
    for (int s = 0; s < 18; ++s) {
      if (s == 17 && w != 0) break;
      const int r = s % PD;
      u32x4 a0 = fa[r][0], a1 = fa[r][1], b0 = fb[r][0], b1 = fb[r][1];
      if (s + PD < 17 || (s + PD == 17 && w == 0)) {
        fa[r][0] = *(const u32x4*)(pa + (s + PD) * 1024);
        fa[r][1] = *(const u32x4*)(pa + (s + PD) * 1024 + NU * 1024);
        fb[r][0] = *(const u32x4*)(pb + (s + PD) * 16384);
        fb[r][1] = *(const u32x4*)(pb + (s + PD) * 16384 + 1024);
      }
      acc[0][0] = mfma32(b0, a0, acc[0][0]);
      acc[0][1] = mfma32(b0, a1, acc[0][1]);
      acc[1][0] = mfma32(b1, a0, acc[1][0]);
      acc[1][1] = mfma32(b1, a1, acc[1][1]);
    }
  } else {
    GAS unsigned* flags = (GAS unsigned*)(ws + WS_FLAG);
    if (w == 7) {
      const int head = b >> 3, sl = b & 7;
      const float* p = (const float*)(ws + WS_KVP) + (size_t)head * 544;
      float s = 0.f, sz = 0.f;
      float t[16], tz[16];
#pragma unroll
      for (int r = 0; r < 16; ++r) t[r] = p[(size_t)r * 32 * 544 + 64 * sl + l];
#pragma unroll
      for (int r = 0; r < 16; ++r) tz[r] = p[(size_t)r * 32 * 544 + 512 + 4 * sl + (l & 3)];
#pragma unroll
      for (int r = 0; r < 16; ++r) { s += t[r]; sz += tz[r]; }
      const float s_n = dpp_f<0xB1>(s), sz_n = dpp_f<0xB1>(sz);
      GAS unsigned* dst = (GAS unsigned*)(ws + WS_KVR + (size_t)head * 1088);
      if ((l & 1) == 0) {
        __hip_atomic_store(dst + 32 * sl + (l >> 1), pk2(s, s_n), __ATOMIC_RELAXED, __HIP_MEMORY_SCOPE_AGENT);
        if (l < 4) __hip_atomic_store(dst + 256 + 2 * sl + (l >> 1), pk2(sz, sz_n), __ATOMIC_RELAXED, __HIP_MEMORY_SCOPE_AGENT);
      }
      WAIT_V(0);
      if (l == 0) __hip_atomic_store(flags + b, 1u, __ATOMIC_RELAXED, __HIP_MEMORY_SCOPE_AGENT);
    }
    const int h0 = (w - 5) * 11;
    const int nh = w == 7 ? 10 : 11;
    const char* pq = ws + WS_PQ + (size_t)(tg0 * 32 + h0) * 2048 + 16 * l;
    const char* pw = ws + WS_WO + (size_t)(h0 * 16 + nt0) * 1024 + 16 * l;
    constexpr int PD = 3;
    u32x4 fq[PD][4], fw[PD][2], fk[PD][2];
#pragma unroll
    for (int s = 0; s < PD; ++s) {
      fq[s][0] = *(const u32x4*)(pq + s * 2048);
      fq[s][1] = *(const u32x4*)(pq + s * 2048 + 1024);
      fq[s][2] = *(const u32x4*)(pq + s * 2048 + 65536);
      fq[s][3] = *(const u32x4*)(pq + s * 2048 + 65536 + 1024);
      fw[s][0] = *(const u32x4*)(pw + s * 16384);
      fw[s][1] = *(const u32x4*)(pw + s * 16384 + 1024);
    }
    __amdgpu_buffer_rsrc_t rs = __builtin_amdgcn_make_buffer_rsrc((void*)(ws + WS_KVR), 0, 0x7fffffff, 0x00020000);
    const int flag_off = (int)(WS_FLAG - WS_KVR) + 16 * l;
    for (int spin = 0; spin < (1 << 22); ++spin) {
      u32x4 f = __builtin_bit_cast(u32x4, __builtin_amdgcn_raw_buffer_load_b128(rs, flag_off, 0, 16));
      const bool ok = (f[0] == 1u) & (f[1] == 1u) & (f[2] == 1u) & (f[3] == 1u);
      if (__all(ok)) break;
      __builtin_amdgcn_s_sleep(2);
      asm volatile("" ::: "memory");
    }
    asm volatile("" ::: "memory");
    const int koff = h0 * 1088 + (l31 < 16 ? (hh * 16 + l31) * 16 : 1024 + hh * 16);
    const int kstep = l31 < 16 ? 512 : 32;
#pragma unroll
    for (int s = 0; s < PD; ++s) {
      fk[s][0] = __builtin_bit_cast(u32x4, __builtin_amdgcn_raw_buffer_load_b128(rs, koff + s * 1088, 0, 16));
      fk[s][1] = __builtin_bit_cast(u32x4, __builtin_amdgcn_raw_buffer_load_b128(rs, koff + s * 1088 + kstep, 0, 16));
    }
#pragma unroll
    for (int s = 0; s < 11; ++s) {
      if (s == 10 && w == 7) break;
      const int r = s % PD;
      u32x4 q0 = fq[r][0], q1 = fq[r][1], q2 = fq[r][2], q3 = fq[r][3], w0 = fw[r][0], w1 = fw[r][1], k0 = fk[r][0], k1 = fk[r][1];
      if (s + PD < 10 || (s + PD == 10 && w != 7)) {
        fk[r][0] = __builtin_bit_cast(u32x4, __builtin_amdgcn_raw_buffer_load_b128(rs, koff + (s + PD) * 1088, 0, 16));
        fk[r][1] = __builtin_bit_cast(u32x4, __builtin_amdgcn_raw_buffer_load_b128(rs, koff + (s + PD) * 1088 + kstep, 0, 16));
        fq[r][0] = *(const u32x4*)(pq + (s + PD) * 2048);
        fq[r][1] = *(const u32x4*)(pq + (s + PD) * 2048 + 1024);
        fq[r][2] = *(const u32x4*)(pq + (s + PD) * 2048 + 65536);
        fq[r][3] = *(const u32x4*)(pq + (s + PD) * 2048 + 65536 + 1024);
        fw[r][0] = *(const u32x4*)(pw + (s + PD) * 16384);
        fw[r][1] = *(const u32x4*)(pw + (s + PD) * 16384 + 1024);
      }
      u32x4 of[2];
#pragma unroll
      for (int g = 0; g < 2; ++g) {
        f32x16 n = {};
        n = mfma32(k0, g ? q2 : q0, n);
        n = mfma32(k1, g ? q3 : q1, n);
        const float rd = __builtin_amdgcn_rcpf(rbf(rbf(n[8]) + 1e-6f));
#pragma unroll
        for (int i = 0; i < 4; ++i) of[g][i] = pk2(rbf(n[2 * i]) * rd, rbf(n[2 * i + 1]) * rd);
      }
      acc[0][0] = mfma32(w0, of[0], acc[0][0]);
      acc[0][1] = mfma32(w0, of[1], acc[0][1]);
      acc[1][0] = mfma32(w1, of[0], acc[1][0]);
      acc[1][1] = mfma32(w1, of[1], acc[1][1]);
    }
    (void)nh;
  }
  {
    LAS char* dst = lds + w * (64 * RED_PITCH) + l31 * RED_PITCH + 16 * hh;
#pragma unroll
    for (int n = 0; n < 2; ++n)
#pragma unroll
      for (int g = 0; g < 2; ++g)
#pragma unroll
        for (int a = 0; a < 4; ++a) {
          f32x4 o = {acc[n][g][4 * a], acc[n][g][4 * a + 1], acc[n][g][4 * a + 2], acc[n][g][4 * a + 3]};
          *(LAS f32x4*)(dst + g * 32 * RED_PITCH + n * 128 + a * 32) = o;
        }
  }
  __syncthreads();
  {
    const LAS char* src = lds + (tid >> 3) * RED_PITCH + 32 * (tid & 7);
    f32x4 f0 = {}, f1 = {}, s0 = {}, s1 = {};
#pragma unroll
    for (int p = 0; p < 5; ++p) {
      f0 += *(const LAS f32x4*)(src + p * 64 * RED_PITCH);
      f1 += *(const LAS f32x4*)(src + p * 64 * RED_PITCH + 16);
    }
#pragma unroll
    for (int p = 5; p < 8; ++p) {
      s0 += *(const LAS f32x4*)(src + p * 64 * RED_PITCH);
      s1 += *(const LAS f32x4*)(src + p * 64 * RED_PITCH + 16);
    }
    float y[8];
#pragma unroll
    for (int j = 0; j < 8; ++j) {
      const float f = rbf(j < 4 ? f0[j & 3] : f1[j & 3]);
      const float s = rbf(j < 4 ? s0[j & 3] : s1[j & 3]);
      const float g = rbf(silu_f(f));
      const float xx = (j & 1) ? hi16(xv[j >> 1]) : lo16(xv[j >> 1]);
      y[j] = xx + rbf(s * g);
    }
    u32x4 o;
#pragma unroll
    for (int i = 0; i < 4; ++i) o[i] = pk2(y[2 * i], y[2 * i + 1]);
    *(u32x4*)(out + xoff) = o;
  }
}

extern "C" void kernel_launch(void* const* d_in, const int* in_sizes, int n_in, void* d_out, int out_size, void* d_ws,
                              size_t ws_size, hipStream_t stream) {
  const u16* x = (const u16*)d_in[0];
  const u16* wqkv = (const u16*)d_in[1];
  const u16* wphi = (const u16*)d_in[2];
  const u16* wo = (const u16*)d_in[3];
  const u16* w1 = (const u16*)d_in[5];
  const u16* w3 = (const u16*)d_in[6];
  const u16* w2 = (const u16*)d_in[7];
  char* ws = (char*)d_ws;
  k1_kernel<<<256, 512, 0, stream>>>(x, wqkv, wphi, wo, w1, w3, w2, ws);
  k2_kernel<<<256, 512, 0, stream>>>(x, ws, (u16*)d_out);
}
```

```cpp
#include <hip/hip_runtime.h>
#include <hip/hip_bf16.h>
#include <stdint.h>
#include <utility>

typedef __bf16 bf16x2 __attribute__((ext_vector_type(2)));
typedef __bf16 bf16x8 __attribute__((ext_vector_type(8)));
typedef float f32x2 __attribute__((ext_vector_type(2)));
typedef float f32x4 __attribute__((ext_vector_type(4)));
typedef float f32x16 __attribute__((ext_vector_type(16)));
typedef short s16x4 __attribute__((ext_vector_type(4)));
typedef unsigned u32x2 __attribute__((ext_vector_type(2)));
typedef unsigned u32x4 __attribute__((ext_vector_type(4)));
typedef unsigned short u16;

#define LAS __attribute__((address_space(3)))
#define GAS __attribute__((address_space(1)))
#if defined(__HIP_DEVICE_COMPILE__)
#pragma clang attribute push(__attribute__((target("no-packed-fp32-ops"))), apply_to = function)
#endif
#define DEVI __device__ __forceinline__

constexpr int S_ = 2048, D_ = 512, F_ = 1376;
constexpr int NU = 86;

constexpr size_t WS_H    = 0;
constexpr size_t WS_PQ   = WS_H + (size_t)64 * NU * 1024;
constexpr size_t WS_W2   = WS_PQ + (size_t)64 * 32 * 2 * 1024;
constexpr size_t WS_WO   = WS_W2 + (size_t)NU * 16 * 1024;
constexpr size_t WS_KVP  = WS_WO + (size_t)32 * 16 * 1024;
constexpr size_t WS_KVR  = WS_KVP + (size_t)16 * 32 * 544 * 4;
constexpr size_t WS_FLAG = WS_KVR + (size_t)32 * 544 * 2;
constexpr size_t WS_XN   = WS_FLAG + 1024;
constexpr size_t WS_WA   = WS_XN + (size_t)64 * 32 * 1024;
static_assert(WS_XN % 1024 == 0, "align");

#define ST(k, i)
#define WAIT_V_(n) asm volatile("s_waitcnt vmcnt(" #n ")" ::: "memory")
#define WAIT_V(n) WAIT_V_(n)
#define WAIT_L0() asm volatile("s_waitcnt lgkmcnt(0)" ::: "memory")
#define BAR() __builtin_amdgcn_s_barrier()

DEVI unsigned f2u(float x) { return __builtin_bit_cast(unsigned, x); }
DEVI float u2f(unsigned x) { return __builtin_bit_cast(float, x); }
DEVI float rbf(float x) {
  f32x2 t = {0.f, x};
  return __builtin_bit_cast(float, __builtin_convertvector(t, bf16x2));
}
DEVI unsigned pk2(float a, float b) {
  f32x2 t = {a, b};
  return __builtin_bit_cast(unsigned, __builtin_convertvector(t, bf16x2));
}
DEVI float lo16(unsigned w) { return __builtin_bit_cast(float, w << 16); }
DEVI float hi16(unsigned w) { return __builtin_bit_cast(float, w & 0xffff0000u); }
DEVI f32x16 mfma32(u32x4 a, u32x4 b, f32x16 c) {
  return __builtin_amdgcn_mfma_f32_32x32x16_bf16(__builtin_bit_cast(bf16x8, a), __builtin_bit_cast(bf16x8, b), c, 0, 0, 0);
}
DEVI u32x2 trread(const LAS char* p) {
  return __builtin_bit_cast(u32x2, __builtin_amdgcn_ds_read_tr16_b64_v4i16((LAS s16x4*)p));
}
DEVI u32x4 trfrag(const LAS char* p, int step) {
  u32x2 a = trread(p), b = trread(p + step);
  u32x4 r = {a[0], a[1], b[0], b[1]};
  return r;
}
DEVI f32x2 xhalf(float v) {
  const unsigned a = f2u(v);
  auto s = __builtin_amdgcn_permlane32_swap(a, a, false, false);
  const unsigned s0 = s[0], s1 = s[1];
  f32x2 r = {u2f(s0), u2f(s1)};
  return r;
}
DEVI float xhalf_max(float v) { f32x2 r = xhalf(v); return fmaxf(r[0], r[1]); }
DEVI float xhalf_sum(float v) { f32x2 r = xhalf(v); return r[0] + r[1]; }
DEVI float max3f(float a, float b, float c) { float r; asm("v_max3_f32 %0, %1, %2, %3" : "=v"(r) : "v"(a), "v"(b), "v"(c)); return r; }
DEVI float max3n(float a, float b, float c) { float r; asm("v_max3_f32 %0, %1, %2, %3\n\ts_nop 1" : "=v"(r) : "v"(a), "v"(b), "v"(c)); return r; }
DEVI float min3f(float a, float b, float c) { float r; asm("v_min3_f32 %0, %1, %2, %3" : "=v"(r) : "v"(a), "v"(b), "v"(c)); return r; }
template <bool MAX> DEVI f32x2 xboth(float a, float b) {
  auto s = __builtin_amdgcn_permlane32_swap(f2u(a), f2u(b), false, false);
  const unsigned s0 = s[0], s1 = s[1];
  const float f0 = u2f(s0), f1 = u2f(s1);
  return xhalf(MAX ? max3n(f0, f1, f1) : f0 + f1);
}
template <int CTRL> DEVI float dpp_f(float v) {
  return __builtin_bit_cast(float, __builtin_amdgcn_update_dpp(0, __builtin_bit_cast(int, v), CTRL, 0xf, 0xf, false));
}
DEVI float sum8(float v) {
  v += dpp_f<0xB1>(v);
  v += dpp_f<0x4E>(v);
  v += dpp_f<0x141>(v);
  return v;
}
DEVI float sum64(float v) {
  v = sum8(v);
  v += dpp_f<0x140>(v);
  const unsigned a = f2u(v);
  auto s = __builtin_amdgcn_permlane16_swap(a, a, false, false);
  const unsigned s0 = s[0], s1 = s[1];
  return xhalf_sum(u2f(s0) + u2f(s1));
}
DEVI float silu_f(float g) { return g * __builtin_amdgcn_rcpf(1.0f + __builtin_amdgcn_exp2f(-1.4426950408889634f * g)); }

DEVI f32x16 fm_p(const f32x16& acc, u32x4 wp, bool upper) {
  u32x4 x0, x1;
#pragma unroll
  for (int i = 0; i < 4; ++i) {
    x0[i] = pk2(acc[2 * i], acc[2 * i + 1]);
    x1[i] = pk2(acc[8 + 2 * i], acc[8 + 2 * i + 1]);
  }
  u32x4 z = {0u, 0u, 0u, 0u};
  u32x4 a0 = upper ? z : wp, a1 = upper ? wp : z;
  f32x16 p = {};
  __builtin_amdgcn_sched_barrier(0);
  __builtin_amdgcn_s_setprio(3);
  p = mfma32(a0, x0, p);
  p = mfma32(a1, x1, p);
  asm volatile("" : "+v"(p));
  __builtin_amdgcn_s_setprio(0);
  __builtin_amdgcn_sched_barrier(0);
  return p;
}
template <int s> DEVI void fm_head(const f32x16& p, u32x4 (&out)[2]) {
  const float L = 1.4426950408889634f;
  {
    float v[8];
#pragma unroll
    for (int j = 0; j < 8; ++j) v[j] = rbf(p[8 * s + j]);
    const float mx = max3n(max3f(v[0], v[1], v[2]), max3f(v[3], v[4], v[5]), max3f(v[6], v[7], v[7]));
    const float mn = min3f(min3f(v[0], v[1], v[2]), min3f(v[3], v[4], v[5]), min3f(v[6], v[7], v[7]));
    const f32x2 mm = xboth<true>(mx, -mn);
    const f32x2 cp = {-L * mm[0], -L * mm[0]}, cn = {-L * mm[1], -L * mm[1]};
    const f32x2 Lp = {L, L}, Ln = {-L, -L};
    f32x2 ep[4], en[4];
#pragma unroll
    for (int i = 0; i < 4; ++i) {
      const f32x2 t = {v[2 * i], v[2 * i + 1]};
      const f32x2 ap = __builtin_elementwise_fma(t, Lp, cp), an = __builtin_elementwise_fma(t, Ln, cn);
      ep[i] = (f32x2){__builtin_amdgcn_exp2f(ap[0]), __builtin_amdgcn_exp2f(ap[1])};
      en[i] = (f32x2){__builtin_amdgcn_exp2f(an[0]), __builtin_amdgcn_exp2f(an[1])};
    }
    const f32x2 sp = (ep[0] + ep[1]) + (ep[2] + ep[3]), sn = (en[0] + en[1]) + (en[2] + en[3]);
    const f32x2 ss = xboth<false>(sp[0] + sp[1], sn[0] + sn[1]);
    const float rp = __builtin_amdgcn_rcpf(ss[0]), rn = __builtin_amdgcn_rcpf(ss[1]);
    const f32x2 rp2 = {rp, rp}, rn2 = {rn, rn};
#pragma unroll
    for (int i = 0; i < 4; ++i) {
      const f32x2 a = ep[i] * rp2, c = en[i] * rn2;
      out[0][i] = pk2(a[0], a[1]);
      out[1][i] = pk2(c[0], c[1]);
    }
  }
}

DEVI u32x4 ffn_epi(const f32x16& acc) {
  u32x4 r;
  const f32x2 nL = {-1.4426950408889634f, -1.4426950408889634f}, one = {1.0f, 1.0f};
#pragma unroll
  for (int i = 0; i < 4; ++i) {
    const f32x2 g = {rbf(acc[2 * i]), rbf(acc[2 * i + 1])}, u = {rbf(acc[8 + 2 * i]), rbf(acc[9 + 2 * i])};
    const f32x2 t = g * nL;
    const f32x2 e = (f32x2){__builtin_amdgcn_exp2f(t[0]), __builtin_amdgcn_exp2f(t[1])} + one;
    const f32x2 sg = g * (f32x2){__builtin_amdgcn_rcpf(e[0]), __builtin_amdgcn_rcpf(e[1])};
    const f32x2 h = (f32x2){rbf(sg[0]), rbf(sg[1])} * u;
    r[i] = pk2(h[0], h[1]);
  }
  return r;
}

constexpr int L_S = 131072;
constexpr int L_PHI = L_S;
constexpr int L_V = L_S + 16384;
constexpr int L_CS = L_S + 24576;
constexpr int L_ST = L_S + 28672;
constexpr int L_ONES = L_S + 30720;

DEVI float dot2acc(unsigned a, unsigned b, float c) {
  return __builtin_amdgcn_fdot2_f32_bf16(__builtin_bit_cast(bf16x2, a), __builtin_bit_cast(bf16x2, b), c, false);
}
DEVI const char* uni(const void* p) {
  const unsigned long long v = (unsigned long long)p;
  const unsigned lo = __builtin_amdgcn_readfirstlane((unsigned)v), hi = __builtin_amdgcn_readfirstlane((unsigned)(v >> 32));
  return (const char*)(((unsigned long long)hi << 32) | lo);
}
#define ONE2 0x3f803f80u
#define RPRIO(n) __builtin_amdgcn_s_setprio(n)
#define SB0() __builtin_amdgcn_sched_barrier(0)
#define STMOD "sc0 sc1"
DEVI void gst(void* p, u32x4 v) { asm volatile("global_store_dwordx4 %0, %1, off " STMOD "\n\ts_nop 1" ::"v"(p), "v"(v) : "memory"); }
#define SGB(mask, n) __builtin_amdgcn_sched_group_barrier(mask, n, 0)

#define GATHER8(r, vo, sb0, sb1, O0, O1, O2, O3)                                                                   \
  asm volatile("global_load_ushort %0, %8, %9 offset:" #O0 "\n\tglobal_load_ushort %1, %8, %9 offset:" #O1          \
               "\n\tglobal_load_ushort %2, %8, %9 offset:" #O2 "\n\tglobal_load_ushort %3, %8, %9 offset:" #O3      \
               "\n\tglobal_load_ushort %4, %8, %10 offset:" #O0 "\n\tglobal_load_ushort %5, %8, %10 offset:" #O1    \
               "\n\tglobal_load_ushort %6, %8, %10 offset:" #O2 "\n\tglobal_load_ushort %7, %8, %10 offset:" #O3    \
               : "=&v"(r[0]), "=&v"(r[1]), "=&v"(r[2]), "=&v"(r[3]), "=&v"(r[4]), "=&v"(r[5]), "=&v"(r[6]), "=&v"(r[7]) \
               : "v"(vo), "s"(sb0), "s"(sb1)                                                                        \
               : "memory")
#define PIN8(r) asm volatile("" : "+v"(r[0]), "+v"(r[1]), "+v"(r[2]), "+v"(r[3]), "+v"(r[4]), "+v"(r[5]), "+v"(r[6]), "+v"(r[7]))

template <int N> struct IC { static constexpr int value = N; };
template <int... I, class F> DEVI void sfor_(std::integer_sequence<int, I...>, F& f) { (f(IC<I>{}), ...); }
template <int N, class F> DEVI void sfor(F f) { sfor_(std::make_integer_sequence<int, N>{}, f); }
DEVI void dma16(unsigned vo, const char* sb, unsigned dst) {
  asm volatile("s_mov_b32 m0, %2\n\ts_nop 0\n\tglobal_load_lds_dwordx4 %0, %1" ::"v"(vo), "s"(sb), "s"(dst) : "memory");
}

__global__ __launch_bounds__(512, 2) void k0_kernel(const u16* __restrict__ x, const u16* __restrict__ wqkv,
                                                    const u16* __restrict__ wo, const u16* __restrict__ w1,
                                                    const u16* __restrict__ w3, const u16* __restrict__ w2,
                                                    char* __restrict__ ws) {
  __shared__ __attribute__((aligned(16))) char lds_raw[8 * 4096 + 8192];
  LAS char* lds = (LAS char*)lds_raw;
  const int b = blockIdx.x, tid = threadIdx.x, l = tid & 63;
  const int w = __builtin_amdgcn_readfirstlane(tid >> 6);
  ST(0, 12);
  const unsigned lw = (unsigned)(uintptr_t)lds + 4096 * w;
  u32x4 xr;
  asm volatile("global_load_dwordx4 %0, %1, off" : "=v"(xr) : "v"(x + (size_t)(b * 8 + w) * 512 + 8 * l) : "memory");
  const int it = w * 256 + b;
  const int g4 = l >> 4, q4 = (l >> 2) & 3, p4 = l & 3;
  int rd0, rstep, fstep;
  size_t dst0, dstep, dstep2 = 0;
  if (it < 704) {
    const int ks = it & 31, u0 = min(4 * (it >> 5), 82);
    const unsigned vo = (unsigned)((l >> 3) * 2752 + 16 * (l & 7));
    const size_t go = (size_t)(16 * ks) * 2752 + u0 * 32;
    const char* s1 = uni((const char*)w1 + go);
    const char* s3 = uni((const char*)w3 + go);
    dma16(vo, s1, lw);
    dma16(vo, s1 + 8 * 2752, lw + 1024);
    dma16(vo, s3, lw + 2048);
    dma16(vo, s3 + 8 * 2752, lw + 3072);
    rd0 = 2048 * (g4 & 1) + (8 * (g4 >> 1) + q4) * 128 + 16 * (p4 & 1) + 8 * (p4 >> 1);
    rstep = 512; fstep = 32;
    dst0 = WS_WA + (size_t)((48 + u0) * 32 + ks) * 1024; dstep = 32768;
  } else if (it < 1560) {
    const char* sb;
    int pitch;
    bool perm = false;
    if (it < 1088) {
      const int j = it - 704, ks = j & 31, cb = j >> 5;
      pitch = 3072; sb = (const char*)wqkv + (size_t)(16 * ks) * 3072 + cb * 256;
      dst0 = WS_WA + (size_t)(4 * cb * 32 + ks) * 1024; dstep = 32768;
    } else {
      const int j = it - 1088, u = j >> 2, cb = j & 3;
      perm = u >= 86;
      pitch = 1024; sb = (perm ? (const char*)wo + (size_t)(u - 86) * 16384 : (const char*)w2 + (size_t)u * 16384) + cb * 256;
      dst0 = WS_W2 + (size_t)(2 * cb * 118 + u) * 2048; dstep = 1024; dstep2 = 118 * 2048 - 2048;
    }
    sb = uni(sb);
    pitch = __builtin_amdgcn_readfirstlane(pitch);
    const unsigned vo = (unsigned)((l >> 4) * pitch + 16 * (l & 15));
#pragma unroll
    for (int i = 0; i < 4; ++i) dma16(vo, sb + (size_t)(4 * i) * pitch, lw + 1024 * i);
    rd0 = ((perm ? 4 : 8) * (g4 >> 1) + q4) * 256 + 32 * (g4 & 1) + 8 * p4;
    rstep = perm ? 2048 : 1024; fstep = 64;
  }
  if (it < 1560) { WAIT_V(4); } else { WAIT_V(0); }
  asm volatile("" : "+v"(xr));
  float v[8];
#pragma unroll
  for (int i = 0; i < 4; ++i) { const unsigned t = xr[i]; v[2 * i] = lo16(t); v[2 * i + 1] = hi16(t); }
  const float mean = sum64(((v[0] + v[1]) + (v[2] + v[3])) + ((v[4] + v[5]) + (v[6] + v[7]))) * (1.0f / 512.0f);
  float sq = 0.f;
#pragma unroll
  for (int i = 0; i < 8; ++i) { const float d = v[i] - mean; sq += d * d; }
  sq = sum64(sq);
  const float mb = rbf(mean);
  const float sd = rbf(sqrtf(rbf(rbf(sq * (1.0f / 512.0f)) + 1e-5f)));
  const float rs = 1.0f / sd;
#pragma unroll
  for (int i = 0; i < 8; ++i) {
    const float d = rbf(v[i] - mb), q = d * rs;
    v[i] = __builtin_fmaf(__builtin_fmaf(-q, sd, d), rs, q);
  }
  u32x4 o;
#pragma unroll
  for (int i = 0; i < 4; ++i) o[i] = pk2(v[2 * i], v[2 * i + 1]);
  *(LAS u32x4*)(lds + 32768 + l * 128 + ((w ^ (l & 7)) * 16)) = o;
  WAIT_L0();
  BAR();
  ST(0, 13);
  {
    const int c16 = tid >> 3, r = tid & 7;
    const u32x4 t = *(const LAS u32x4*)(lds + 32768 + c16 * 128 + ((r ^ (c16 & 7)) * 16));
    gst(ws + WS_XN + (size_t)(b >> 2) * 32768 + c16 * 512 + ((b & 3) * 8 + r) * 16, t);
  }
  if (it < 1560) {
    WAIT_V(1);
    const LAS char* p = lds + 4096 * w + rd0;
    u32x4 f[4];
#pragma unroll
    for (int i = 0; i < 4; ++i) f[i] = trfrag(p + fstep * i, rstep);
#pragma unroll
    for (int i = 0; i < 4; ++i) gst(ws + dst0 + dstep * i + dstep2 * (i >> 1) + 16 * l, f[i]);
  }
  if (tid < 34) ((unsigned*)(ws + WS_KVR))[b * 34 + tid] = 0xffffffffu;
  ST(0, 14);
}

__global__ __launch_bounds__(512, 2) void k1_kernel(const u16* __restrict__ wphi, char* __restrict__ ws) {
  __shared__ __attribute__((aligned(16))) char lds_raw[163840];
  LAS char* lds = (LAS char*)lds_raw;
  const int b = blockIdx.x, tid = threadIdx.x, l = tid & 63;
  const int w = __builtin_amdgcn_readfirstlane(tid >> 6);
  const int xcd = b & 7, idx = b >> 3;
  const int rb = (xcd & 1) * 8 + (idx & 7);
  const int c = (xcd >> 1) * 4 + (idx >> 3);
  const int nfb = c < 11 ? 3 : 2;
  const int fb0 = c < 11 ? 3 * c : 33 + 2 * (c - 11);
  const int rg = w & 3, hw = w >> 2;
  const int hh = l >> 5, l31 = l & 31;
  const int tg = rb * 4 + rg;
  ST(0, 0);

  const unsigned ldw = (unsigned)(uintptr_t)lds + 4096 * w;
  const unsigned vo = 16 * l;
  const char* sb_kv = uni(ws + WS_WA + (size_t)((16 * (1 + hw) + c) * 32 + 4 * rg) * 1024);
  const char* sb_q = uni(ws + WS_WA + (size_t)(c * 32 + 4 * w) * 1024);
  const char* sb_f = uni(ws + WS_WA + (size_t)((48 + 2 * fb0 + hw) * 32 + 4 * rg) * 1024);
  const char* sb_x = uni(ws + WS_XN + (size_t)((rb * 4 + (w >> 1)) * 32 + 8 * (w & 1)) * 1024);
  auto dma4 = [&](const char* sb, int slot) {
#pragma unroll
    for (int i = 0; i < 4; ++i) dma16(vo, sb + 1024 * i, ldw + slot + 1024 * i);
  };
  auto dma_kv = [&](int slot, int khalf) { dma4(sb_kv + khalf * 16384, slot); };
  auto dma_q = [&](int slot) { dma4(sb_q, slot); };
  auto dma_f = [&](int slot, int blk, int khalf) { dma4(sb_f + blk * 65536 + khalf * 16384, slot); };
  auto dma_x = [&](int khalf) {
#pragma unroll
    for (int i = 0; i < 8; ++i) dma16(vo, sb_x + khalf * 16384 + 1024 * i, ldw + 4096 * w + khalf * 65536 + 1024 * i);
  };
  dma_x(0);
  dma_kv(L_S, 0);
  BAR();
  u32x4 xa[32];
  {
    const char* sx = uni(ws + WS_XN + (size_t)(tg * 32 + 16) * 1024);
#pragma unroll
    for (int t = 0; t < 16; t += 4)
      asm volatile("global_load_dwordx4 %0, %4, %5 offset:0\n\tglobal_load_dwordx4 %1, %4, %5 offset:1024\n\t"
                   "global_load_dwordx4 %2, %4, %5 offset:2048\n\tglobal_load_dwordx4 %3, %4, %5 offset:3072"
                   : "=&v"(xa[16 + t]), "=&v"(xa[17 + t]), "=&v"(xa[18 + t]), "=&v"(xa[19 + t])
                   : "v"(vo), "s"(sx + 1024 * t) : "memory");
  }
  dma_kv(65536, 1);
  dma_q(98304);
  unsigned wg[8];
  {
    const unsigned go = (unsigned)(((l31 >> 4) * 256 + (l31 & 15) + 64 * hh) * 2);
    const char* sb = uni((const char*)wphi + c * 1024);
    GATHER8(wg, go, sb, sb + 256, 0, 32, 64, 96);
  }
  const int g4 = l >> 4, q4 = (l >> 2) & 3, p4 = l & 3;
  const int krow = 8 * (g4 >> 1) + q4;
  const int off_q = krow * 64 + 32 * (g4 & 1) + 8 * p4;
  const LAS char* pw = lds + 16384 * hw + 16 * l;
  auto wfrag = [&](const LAS char* p, int t) { return *(const LAS u32x4*)(p + 1024 * t); };
  const int tok = 32 * rg + l31;
  const LAS char* px = lds + 16384 * rg + 16 * l;

  WAIT_V(32);
  BAR();
  ST(0, 1);
#pragma unroll
  for (int t = 0; t < 16; ++t) xa[t] = wfrag(px, t);
  u32x4 fr[4];
  auto pre = [&](const LAS char* p) {
    SB0();
#pragma unroll
    for (int i = 0; i < 4; ++i) fr[i] = wfrag(p, i);
    SB0();
  };
  pre(pw + L_S);
  WAIT_L0();
  BAR();
  ST(0, 2);
  auto run = [&](f32x16& a, const LAS char* p, int x0, auto NC, auto DC, const char* src, unsigned dst, auto side) {
    constexpr int n = decltype(NC)::value, nd = decltype(DC)::value, ev = n / (nd ? nd : 1);
    RPRIO(2);
    SB0();
    sfor<n>([&](auto TC) {
      constexpr int t = decltype(TC)::value;
      a = mfma32(fr[t & 3], xa[x0 + t], a);
      asm volatile("" : "+v"(a));
      if (t + 4 < n) fr[t & 3] = wfrag(p, t + 4);
      if (nd && t % ev == 0) dma16(vo, src + 1024 * (t / ev), dst + 1024 * (t / ev));
      side(TC);
      SB0();
    });
    RPRIO(0);
  };
  auto none = [](auto) {};
  const LAS char* pu = lds + 16 * l;
  const char* su = uni(ws + WS_WA + (size_t)((48 + 2 * fb0) * 32 + 8 * rg) * 1024);
  const unsigned du = (unsigned)(uintptr_t)lds + 8192 * rg;
  char* hdst = ws + WS_H + (size_t)(tg * NU + 2 * fb0) * 1024 + 16 * l;
  f32x16 acc = {}, a2 = {};
  float eg, eu, et, eh0;
  u32x4 er;
  auto epi_side = [&](auto TC) {
    constexpr int t = decltype(TC)::value, j = t >> 2, st = t & 3;
    if (st == 0) { eg = rbf(acc[j]); eu = rbf(acc[8 + j]); et = eg * -1.4426950408889634f; }
    if (st == 1) { et = __builtin_amdgcn_exp2f(et) + 1.0f; }
    if (st == 2) { et = eg * __builtin_amdgcn_rcpf(et); }
    if (st == 3) {
      et = rbf(et) * eu;
      if (j & 1) er[j >> 1] = pk2(eh0, et); else eh0 = et;
    }
    asm volatile("" : "+v"(et));
  };
  run(acc, pw + L_S, 0, IC<16>{}, IC<8>{}, su + (hw ? 0 : 32768), du + 32768 * hw, none);
  WAIT_V(20);
  BAR();
  ST(0, 3);
  asm volatile("" : "+v"(xa[16]), "+v"(xa[17]), "+v"(xa[18]), "+v"(xa[19]), "+v"(xa[20]), "+v"(xa[21]), "+v"(xa[22]), "+v"(xa[23]));
  asm volatile("" : "+v"(xa[24]), "+v"(xa[25]), "+v"(xa[26]), "+v"(xa[27]), "+v"(xa[28]), "+v"(xa[29]), "+v"(xa[30]), "+v"(xa[31]));
  pre(pw + 65536);
  run(acc, pw + 65536, 16, IC<16>{}, IC<0>{}, su, du, none);
  WAIT_V(8);
  PIN8(wg);
  u32x4 wp;
#pragma unroll
  for (int i = 0; i < 4; ++i) wp[i] = wg[2 * i] | (wg[2 * i + 1] << 16);
  if (hw == 0) {
    if (tid < 8) {
      u32x4 ones = {ONE2, ONE2, ONE2, ONE2};
      *(LAS u32x4*)(lds + L_ONES + 16 * tid) = ones;
    }
    BAR();
    ST(0, 4);
    LAS char* pimg = lds + L_PHI + tok * 64 + hh * 16;
    const f32x16 p = fm_p(acc, wp, l31 >= 16);
    u32x4 ph[2];
    fm_head<0>(p, ph);
    *(LAS u32x4*)(pimg) = ph[0];
    *(LAS u32x4*)(pimg + 32) = ph[1];
    WAIT_V(0);
    BAR();
    ST(0, 5);
    fm_head<1>(p, ph);
    *(LAS u32x4*)(pimg + 8192) = ph[0];
    *(LAS u32x4*)(pimg + 8192 + 32) = ph[1];
    pre(pu + 32768);
    BAR();
    ST(0, 6);
    acc = (f32x16){};
    if (nfb == 3) run(acc, pu + 32768, 0, IC<32>{}, IC<8>{}, su + 4 * 32768, du, none);
    else run(acc, pu + 32768, 0, IC<32>{}, IC<0>{}, su, du, none);
    pre(pu + 65536);
    BAR();
    ST(0, 7);
    if (nfb == 3) { run(a2, pu + 65536, 0, IC<32>{}, IC<8>{}, su + 5 * 32768, du + 32768, epi_side); WAIT_V(8); }
    else run(a2, pu + 65536, 0, IC<32>{}, IC<0>{}, su, du, epi_side);
    gst(hdst, er);
    BAR();
    ST(0, 8);
    gst(hdst + 2048, ffn_epi(a2));
    if (w < 2) {
      f32x16 kv = {};
      const LAS char* pa = lds + L_PHI + w * 8192 + off_q;
      const LAS char* pb = (g4 & 1) ? (lds + L_ONES + q4 * 32 + 8 * p4) : (lds + L_V + krow * 64 + 32 * w + 8 * p4);
      const int bstep = (g4 & 1) ? 0 : 1024, b4 = (g4 & 1) ? 0 : 256;
#pragma unroll
      for (int h2 = 0; h2 < 2; ++h2) {
        u32x4 ka[4], kb[4];
#pragma unroll
        for (int t = 0; t < 4; ++t) { ka[t] = trfrag(pa + 1024 * (4 * h2 + t), 256); kb[t] = trfrag(pb + bstep * (4 * h2 + t), b4); }
        WAIT_L0();
        SB0();
        RPRIO(3);
#pragma unroll
        for (int t = 0; t < 4; ++t) kv = mfma32(ka[t], kb[t], kv);
        asm volatile("" : "+v"(kv));
        RPRIO(0);
        SB0();
      }
      float* dst = (float*)(ws + WS_KVP) + (size_t)(rb * 32 + 2 * c + w) * 544;
      if (l31 <= 16) {
#pragma unroll
        for (int a = 0; a < 4; ++a) {
          f32x4 o = {kv[4 * a], kv[4 * a + 1], kv[4 * a + 2], kv[4 * a + 3]};
          const int e = l31 < 16 ? (a * 16 + l31) * 8 + 4 * hh : 512 + 8 * a + 4 * hh;
          gst(dst + e, __builtin_bit_cast(u32x4, o));
        }
      }
    }
    if (nfb == 3) {
      pre(pu);
      if (w < 2) { WAIT_V(6); } else { WAIT_V(2); }
      BAR();
      ST(0, 9);
      acc = (f32x16){};
      run(acc, pu, 0, IC<32>{}, IC<0>{}, su, du, none);
      ST(0, 10);
      gst(hdst + 4096, ffn_epi(acc));
    }
  } else {
    BAR();
    ST(0, 4);
    pre(pu + 98304);
    run(a2, pu + 98304, 0, IC<32>{}, IC<8>{}, su + 2 * 32768, du + 65536, [&](auto TC) {
      constexpr int t = decltype(TC)::value;
      if (t < 4) {
        u32x2 o = {pk2(acc[4 * t], acc[4 * t + 1]), pk2(acc[4 * t + 2], acc[4 * t + 3])};
        *(LAS u32x2*)(lds + L_V + tok * 64 + (8 * t + 4 * hh) * 2) = o;
      }
    });
    WAIT_V(8);
    BAR();
    ST(0, 5);
    acc = (f32x16){};
    pre(pu);
    run(acc, pu, 0, IC<32>{}, IC<8>{}, su + 3 * 32768, du + 98304, none);
    WAIT_V(8);
    BAR();
    ST(0, 6);
    char* dst = ws + WS_PQ + (size_t)(tg * 32 + 2 * c) * 2048 + 16 * l;
    const f32x16 p = fm_p(a2, wp, l31 >= 16);
    u32x4 ph[2];
    fm_head<0>(p, ph);
    gst(dst, ph[0]);
    gst(dst + 1024, ph[1]);
    WAIT_V(2);
    BAR();
    ST(0, 7);
    fm_head<1>(p, ph);
    gst(dst + 2048, ph[0]);
    gst(dst + 3072, ph[1]);
    pre(pu + 98304);
    BAR();
    ST(0, 8);
    a2 = (f32x16){};
    run(a2, pu + 98304, 0, IC<32>{}, IC<0>{}, su, du, epi_side);
    gst(hdst + 1024, er);
    if (nfb == 3) {
      BAR();
      ST(0, 9);
    }
    gst(hdst + 3072, ffn_epi(a2));
    if (nfb == 3) {
      ST(0, 10);
      acc = (f32x16){};
      pre(pu + 32768);
      run(acc, pu + 32768, 0, IC<32>{}, IC<0>{}, su, du, none);
      gst(hdst + 5120, ffn_epi(acc));
    }
  }
  ST(0, 11);
}

#define FPD 4
#define KWAIT 0
#define PRIO(n) __builtin_amdgcn_s_setprio(n)
constexpr int RED_PITCH = 272;

__global__ __launch_bounds__(512, 2) void k2_kernel(const u16* __restrict__ x, char* ws, u16* __restrict__ out) {
  __shared__ __attribute__((aligned(16))) char lds_raw[8 * 64 * RED_PITCH];
  LAS char* lds = (LAS char*)lds_raw;
  const int b = blockIdx.x, tid = threadIdx.x, l = tid & 63;
  const int w = __builtin_amdgcn_readfirstlane(tid >> 6);
  const int xcd = b & 7, idx = b >> 3;
  const int rb = xcd * 4 + (idx & 3);
  const int ct = idx >> 2;
  const int hh = l >> 5, l31 = l & 31;
  const int tg0 = 2 * rb, nt0 = 2 * ct;
  const bool prod = (w == 0) && (b < 136);
  ST(1, 0);
  f32x4 pt[8];
  if (prod) {
    const f32x4* p = (const f32x4*)(ws + WS_KVP) + (size_t)hh * 8 * 4352 + b * 32 + l31;
#pragma unroll
    for (int r = 0; r < 8; ++r) pt[r] = p[(size_t)r * 4352];
  }
  SB0();
  BAR();
  SB0();
  const size_t xoff = (size_t)(rb * 64 + (tid >> 3)) * 512 + ct * 64 + 8 * (tid & 7);
  u32x4 xv;
  f32x16 acc[2][2] = {};
  if (w >= 4) {
    const int u0 = w < 6 ? 22 * (w - 4) : 44 + 21 * (w - 6);
    const bool full = w < 6;
    const char* pa = ws + WS_H + (size_t)(tg0 * NU + u0) * 1024 + 16 * l;
    const char* pb = ws + WS_W2 + (size_t)(ct * 118 + u0) * 2048 + 16 * l;
    constexpr int PD = FPD;
    u32x4 fa[PD][2], fb[PD][2];
#pragma unroll
    for (int s = 0; s < PD; ++s) {
      fa[s][0] = *(const u32x4*)(pa + s * 1024);
      fa[s][1] = *(const u32x4*)(pa + s * 1024 + NU * 1024);
      fb[s][0] = *(const u32x4*)(pb + s * 2048);
      fb[s][1] = *(const u32x4*)(pb + s * 2048 + 1024);
    }
    SB0();
    sfor<22>([&](auto SC) {
      constexpr int s = decltype(SC)::value, r = s % PD;
      if (s < 21 || full) {
        acc[0][0] = mfma32(fb[r][0], fa[r][0], acc[0][0]);
        acc[0][1] = mfma32(fb[r][0], fa[r][1], acc[0][1]);
        acc[1][0] = mfma32(fb[r][1], fa[r][0], acc[1][0]);
        acc[1][1] = mfma32(fb[r][1], fa[r][1], acc[1][1]);
      }
      SB0();
      if (s + PD < 21 || (s + PD == 21 && full)) {
        fa[r][0] = *(const u32x4*)(pa + (s + PD) * 1024);
        fa[r][1] = *(const u32x4*)(pa + (s + PD) * 1024 + NU * 1024);
        fb[r][0] = *(const u32x4*)(pb + (s + PD) * 2048);
        fb[r][1] = *(const u32x4*)(pb + (s + PD) * 2048 + 1024);
      }
      if (s + PD == 22) xv = *(const u32x4*)(x + xoff);
      SB0();
    });
  } else {
    PRIO(2);
    const char* pq = ws + WS_PQ + (size_t)(tg0 * 32 + 8 * w) * 2048 + 16 * l;
    const char* pw = ws + WS_W2 + (size_t)(ct * 118 + 86 + 8 * w) * 2048 + 16 * l;
    const unsigned kdst = (unsigned)(uintptr_t)lds + w * (64 * RED_PITCH);
    const char* ksrc = uni(ws + WS_KVR + w * 8704);
    const LAS char* pk = lds + w * (64 * RED_PITCH) + (l31 < 16 ? (hh * 16 + l31) * 16 : 1024 + hh * 16);
    const int kstep = l31 < 16 ? 512 : 32;
    auto dmaK = [&]() {
      const unsigned vo = 16 * l;
#pragma unroll
      for (int i = 0; i < 8; ++i)
        asm volatile("s_mov_b32 m0, %2\n\ts_nop 0\n\tglobal_load_lds_dwordx4 %0, %1 sc1" ::"v"(vo), "s"(ksrc + 1024 * i), "s"(kdst + 1024 * i) : "memory");
      if (l < 32)
        asm volatile("s_mov_b32 m0, %2\n\ts_nop 0\n\tglobal_load_lds_dwordx4 %0, %1 sc1" ::"v"(vo), "s"(ksrc + 8192), "s"(kdst + 8192) : "memory");
    };
    u32x4 fq[8][4], fw[8][2], fk[8][2];
    f32x16 n[16];
    auto ldQ = [&](auto SC) {
      constexpr int s = decltype(SC)::value;
      fq[s][0] = *(const u32x4*)(pq + s * 2048);
      fq[s][1] = *(const u32x4*)(pq + s * 2048 + 1024);
      fq[s][2] = *(const u32x4*)(pq + s * 2048 + 65536);
      fq[s][3] = *(const u32x4*)(pq + s * 2048 + 65536 + 1024);
      fw[s][0] = *(const u32x4*)(pw + s * 2048);
      fw[s][1] = *(const u32x4*)(pw + s * 2048 + 1024);
      SB0();
    };
    auto rdK = [&](auto SC) {
      constexpr int s = decltype(SC)::value;
      fk[s][0] = *(const LAS u32x4*)(pk + s * 1088);
      fk[s][1] = *(const LAS u32x4*)(pk + s * 1088 + kstep);
    };
    auto num = [&](auto UC) {
      constexpr int u = decltype(UC)::value, s = u >> 1, g = u & 1;
      if (g == 0) {
        for (int spin = 0; spin < (1 << 16); ++spin) {
          const unsigned m0 = max(max(fk[s][0][0], fk[s][0][1]), max(fk[s][0][2], fk[s][0][3]));
          const unsigned m1 = max(max(fk[s][1][0], fk[s][1][1]), max(fk[s][1][2], fk[s][1][3]));
          if (__builtin_amdgcn_ballot_w64(max(m0, m1) == 0xffffffffu) == 0) break;
          __builtin_amdgcn_s_sleep(4);
          dmaK();
          WAIT_V(0);
          rdK(IC<s>{});
        }
        if (s < 7) rdK(IC<(s + 1) & 7>{});
      }
      const f32x16 z = {};
      n[u] = mfma32(fk[s][0], fq[s][2 * g], z);
      n[u] = mfma32(fk[s][1], fq[s][2 * g + 1], n[u]);
      SB0();
    };
    auto proj = [&](auto UC) {
      constexpr int u = decltype(UC)::value, s = u >> 1, g = u & 1;
      u32x4 of;
      const float rd = __builtin_amdgcn_rcpf(rbf(rbf(n[u][8]) + 1e-6f));
#pragma unroll
      for (int i = 0; i < 4; ++i) of[i] = pk2(rbf(n[u][2 * i]) * rd, rbf(n[u][2 * i + 1]) * rd);
      acc[0][g] = mfma32(fw[s][0], of, acc[0][g]);
      acc[1][g] = mfma32(fw[s][1], of, acc[1][g]);
      SB0();
    };
    ldQ(IC<0>{}); ldQ(IC<1>{});
    if (prod) {
      f32x4 sacc = pt[0];
#pragma unroll
      for (int r = 1; r < 8; ++r) sacc += pt[r];
      float s4[4];
#pragma unroll
      for (int e = 0; e < 4; ++e) { const float v = sacc[e]; s4[e] = xhalf_sum(v); }
      const unsigned long long v = (unsigned long long)pk2(s4[0], s4[1]) | ((unsigned long long)pk2(s4[2], s4[3]) << 32);
      if (l < 32) __hip_atomic_store((GAS unsigned long long*)(ws + WS_KVR) + b * 32 + l31, v, __ATOMIC_RELAXED, __HIP_MEMORY_SCOPE_AGENT);
      ST(1, 1);
    }
    SB0();
    WAIT_V(7); SB0();
    ldQ(IC<2>{});
    WAIT_V(7); SB0();
    ldQ(IC<3>{});
    WAIT_V(KWAIT); SB0();
    ST(1, 2);
    dmaK();
    ldQ(IC<4>{});
    WAIT_V(6); SB0();
    rdK(IC<0>{});
    num(IC<0>{});
    ST(1, 6);
    sfor<16>([&](auto UC) {
      constexpr int u = decltype(UC)::value;
      if (u < 15) num(IC<(u + 1) & 15>{});
      proj(UC);
      if ((u & 1) && (u >> 1) + 5 < 8) ldQ(IC<((u >> 1) + 5) & 7>{});
      if (u == 7) { xv = *(const u32x4*)(x + xoff); SB0(); }
    });
    WAIT_L0();
  }
  ST(1, 3);
  {
    LAS char* dst = lds + w * (64 * RED_PITCH) + l31 * RED_PITCH + 16 * hh;
#pragma unroll
    for (int nn = 0; nn < 2; ++nn)
#pragma unroll
      for (int g = 0; g < 2; ++g)
#pragma unroll
        for (int a = 0; a < 4; ++a) {
          f32x4 o = {acc[nn][g][4 * a], acc[nn][g][4 * a + 1], acc[nn][g][4 * a + 2], acc[nn][g][4 * a + 3]};
          *(LAS f32x4*)(dst + g * 32 * RED_PITCH + nn * 128 + a * 32) = o;
        }
  }
  WAIT_L0();
  BAR();
  ST(1, 4);
  {
    const LAS char* src = lds + (tid >> 3) * RED_PITCH + 32 * (tid & 7);
    f32x4 f0 = {}, f1 = {}, s0 = {}, s1 = {};
#pragma unroll
    for (int p = 0; p < 4; ++p) {
      s0 += *(const LAS f32x4*)(src + p * 64 * RED_PITCH);
      s1 += *(const LAS f32x4*)(src + p * 64 * RED_PITCH + 16);
      f0 += *(const LAS f32x4*)(src + (p + 4) * 64 * RED_PITCH);
      f1 += *(const LAS f32x4*)(src + (p + 4) * 64 * RED_PITCH + 16);
    }
    float y[8];
#pragma unroll
    for (int j = 0; j < 8; ++j) {
      const float f = rbf(j < 4 ? f0[j & 3] : f1[j & 3]);
      const float sv = rbf(j < 4 ? s0[j & 3] : s1[j & 3]);
      const float g = rbf(silu_f(f));
      const float xx = (j & 1) ? hi16(xv[j >> 1]) : lo16(xv[j >> 1]);
      y[j] = xx + rbf(sv * g);
    }
    u32x4 o;
#pragma unroll
    for (int i = 0; i < 4; ++i) o[i] = pk2(y[2 * i], y[2 * i + 1]);
    *(u32x4*)(out + xoff) = o;
  }
  ST(1, 5);
}

#if defined(__HIP_DEVICE_COMPILE__)
#pragma clang attribute pop
#endif

extern "C" void kernel_launch(void* const* d_in, const int* in_sizes, int n_in, void* d_out, int out_size, void* d_ws,
                              size_t ws_size, hipStream_t stream) {
  const u16* x = (const u16*)d_in[0];
  const u16* wqkv = (const u16*)d_in[1];
  const u16* wphi = (const u16*)d_in[2];
  const u16* wo = (const u16*)d_in[3];
  const u16* w1 = (const u16*)d_in[5];
  const u16* w3 = (const u16*)d_in[6];
  const u16* w2 = (const u16*)d_in[7];
  char* ws = (char*)d_ws;
  k0_kernel<<<256, 512, 0, stream>>>(x, wqkv, wo, w1, w3, w2, ws);
  k1_kernel<<<256, 512, 0, stream>>>(wphi, ws);
  k2_kernel<<<256, 512, 0, stream>>>(x, ws, (u16*)d_out);
}
```

```cpp
#include <hip/hip_runtime.h>
#include <hip/hip_bf16.h>
#include <stdint.h>
#include <utility>

typedef __bf16 bf16x2 __attribute__((ext_vector_type(2)));
typedef __bf16 bf16x8 __attribute__((ext_vector_type(8)));
typedef float f32x2 __attribute__((ext_vector_type(2)));
typedef float f32x4 __attribute__((ext_vector_type(4)));
typedef float f32x16 __attribute__((ext_vector_type(16)));
typedef short s16x4 __attribute__((ext_vector_type(4)));
typedef unsigned u32x2 __attribute__((ext_vector_type(2)));
typedef unsigned u32x4 __attribute__((ext_vector_type(4)));
typedef unsigned short u16;

#define LAS __attribute__((address_space(3)))
#define GAS __attribute__((address_space(1)))
#if defined(__HIP_DEVICE_COMPILE__)
#pragma clang attribute push(__attribute__((target("no-packed-fp32-ops"))), apply_to = function)
#endif
#define DEVI __device__ __forceinline__

constexpr int S_ = 2048, D_ = 512, F_ = 1376;
constexpr int NU = 86;

constexpr size_t WS_H    = 0;
constexpr size_t WS_PQ   = WS_H + (size_t)64 * NU * 1024;
constexpr size_t WS_W2   = WS_PQ + (size_t)64 * 32 * 2 * 1024;
constexpr size_t WS_WO   = WS_W2 + (size_t)NU * 16 * 1024;
constexpr size_t WS_KVP  = WS_WO + (size_t)32 * 16 * 1024;
constexpr size_t WS_KVR  = WS_KVP + (size_t)16 * 32 * 544 * 4;
constexpr size_t WS_FLAG = WS_KVR + (size_t)32 * 544 * 2;
constexpr size_t WS_XN   = WS_FLAG + 1024;
constexpr size_t WS_WA   = WS_XN + (size_t)64 * 32 * 1024;
static_assert(WS_XN % 1024 == 0, "align");

#define ST(k, i)
#define WAIT_V_(n) asm volatile("s_waitcnt vmcnt(" #n ")" ::: "memory")
#define WAIT_V(n) WAIT_V_(n)
#define WAIT_L0() asm volatile("s_waitcnt lgkmcnt(0)" ::: "memory")
#define BAR() __builtin_amdgcn_s_barrier()

DEVI unsigned f2u(float x) { return __builtin_bit_cast(unsigned, x); }
DEVI float u2f(unsigned x) { return __builtin_bit_cast(float, x); }
DEVI float rbf(float x) {
  f32x2 t = {0.f, x};
  return __builtin_bit_cast(float, __builtin_convertvector(t, bf16x2));
}
DEVI unsigned pk2(float a, float b) {
  f32x2 t = {a, b};
  return __builtin_bit_cast(unsigned, __builtin_convertvector(t, bf16x2));
}
DEVI float lo16(unsigned w) { return __builtin_bit_cast(float, w << 16); }
DEVI float hi16(unsigned w) { return __builtin_bit_cast(float, w & 0xffff0000u); }
DEVI f32x16 mfma32(u32x4 a, u32x4 b, f32x16 c) {
  return __builtin_amdgcn_mfma_f32_32x32x16_bf16(__builtin_bit_cast(bf16x8, a), __builtin_bit_cast(bf16x8, b), c, 0, 0, 0);
}
DEVI u32x2 trread(const LAS char* p) {
  return __builtin_bit_cast(u32x2, __builtin_amdgcn_ds_read_tr16_b64_v4i16((LAS s16x4*)p));
}
DEVI u32x4 trfrag(const LAS char* p, int step) {
  u32x2 a = trread(p), b = trread(p + step);
  u32x4 r = {a[0], a[1], b[0], b[1]};
  return r;
}
DEVI f32x2 xhalf(float v) {
  const unsigned a = f2u(v);
  auto s = __builtin_amdgcn_permlane32_swap(a, a, false, false);
  const unsigned s0 = s[0], s1 = s[1];
  f32x2 r = {u2f(s0), u2f(s1)};
  return r;
}
DEVI float xhalf_max(float v) { f32x2 r = xhalf(v); return fmaxf(r[0], r[1]); }
DEVI float xhalf_sum(float v) { f32x2 r = xhalf(v); return r[0] + r[1]; }
DEVI float max3f(float a, float b, float c) { float r; asm("v_max3_f32 %0, %1, %2, %3" : "=v"(r) : "v"(a), "v"(b), "v"(c)); return r; }
DEVI float max3n(float a, float b, float c) { float r; asm("v_max3_f32 %0, %1, %2, %3\n\ts_nop 1" : "=v"(r) : "v"(a), "v"(b), "v"(c)); return r; }
DEVI float min3f(float a, float b, float c) { float r; asm("v_min3_f32 %0, %1, %2, %3" : "=v"(r) : "v"(a), "v"(b), "v"(c)); return r; }
template <bool MAX> DEVI f32x2 xboth(float a, float b) {
  auto s = __builtin_amdgcn_permlane32_swap(f2u(a), f2u(b), false, false);
  const unsigned s0 = s[0], s1 = s[1];
  const float f0 = u2f(s0), f1 = u2f(s1);
  return xhalf(MAX ? max3n(f0, f1, f1) : f0 + f1);
}
template <int CTRL> DEVI float dpp_f(float v) {
  return __builtin_bit_cast(float, __builtin_amdgcn_update_dpp(0, __builtin_bit_cast(int, v), CTRL, 0xf, 0xf, false));
}
DEVI float sum8(float v) {
  v += dpp_f<0xB1>(v);
  v += dpp_f<0x4E>(v);
  v += dpp_f<0x141>(v);
  return v;
}
DEVI float sum64(float v) {
  v = sum8(v);
  v += dpp_f<0x140>(v);
  const unsigned a = f2u(v);
  auto s = __builtin_amdgcn_permlane16_swap(a, a, false, false);
  const unsigned s0 = s[0], s1 = s[1];
  return xhalf_sum(u2f(s0) + u2f(s1));
}
DEVI float silu_f(float g) { return g * __builtin_amdgcn_rcpf(1.0f + __builtin_amdgcn_exp2f(-1.4426950408889634f * g)); }

DEVI f32x16 fm_p(const f32x16& acc, u32x4 wp, bool upper) {
  u32x4 x0, x1;
#pragma unroll
  for (int i = 0; i < 4; ++i) {
    x0[i] = pk2(acc[2 * i], acc[2 * i + 1]);
    x1[i] = pk2(acc[8 + 2 * i], acc[8 + 2 * i + 1]);
  }
  u32x4 z = {0u, 0u, 0u, 0u};
  u32x4 a0 = upper ? z : wp, a1 = upper ? wp : z;
  f32x16 p = {};
  __builtin_amdgcn_sched_barrier(0);
  __builtin_amdgcn_s_setprio(3);
  p = mfma32(a0, x0, p);
  p = mfma32(a1, x1, p);
  asm volatile("" : "+v"(p));
  __builtin_amdgcn_s_setprio(0);
  __builtin_amdgcn_sched_barrier(0);
  return p;
}
template <int s> DEVI void fm_head(const f32x16& p, u32x4 (&out)[2]) {
  const float L = 1.4426950408889634f;
  {
    float v[8];
#pragma unroll
    for (int j = 0; j < 8; ++j) v[j] = rbf(p[8 * s + j]);
    const float mx = max3n(max3f(v[0], v[1], v[2]), max3f(v[3], v[4], v[5]), max3f(v[6], v[7], v[7]));
    const float mn = min3f(min3f(v[0], v[1], v[2]), min3f(v[3], v[4], v[5]), min3f(v[6], v[7], v[7]));
    const f32x2 mm = xboth<true>(mx, -mn);
    const f32x2 cp = {-L * mm[0], -L * mm[0]}, cn = {-L * mm[1], -L * mm[1]};
    const f32x2 Lp = {L, L}, Ln = {-L, -L};
    f32x2 ep[4], en[4];
#pragma unroll
    for (int i = 0; i < 4; ++i) {
      const f32x2 t = {v[2 * i], v[2 * i + 1]};
      const f32x2 ap = __builtin_elementwise_fma(t, Lp, cp), an = __builtin_elementwise_fma(t, Ln, cn);
      ep[i] = (f32x2){__builtin_amdgcn_exp2f(ap[0]), __builtin_amdgcn_exp2f(ap[1])};
      en[i] = (f32x2){__builtin_amdgcn_exp2f(an[0]), __builtin_amdgcn_exp2f(an[1])};
    }
    const f32x2 sp = (ep[0] + ep[1]) + (ep[2] + ep[3]), sn = (en[0] + en[1]) + (en[2] + en[3]);
    const f32x2 ss = xboth<false>(sp[0] + sp[1], sn[0] + sn[1]);
    const float rp = __builtin_amdgcn_rcpf(ss[0]), rn = __builtin_amdgcn_rcpf(ss[1]);
    const f32x2 rp2 = {rp, rp}, rn2 = {rn, rn};
#pragma unroll
    for (int i = 0; i < 4; ++i) {
      const f32x2 a = ep[i] * rp2, c = en[i] * rn2;
      out[0][i] = pk2(a[0], a[1]);
      out[1][i] = pk2(c[0], c[1]);
    }
  }
}

DEVI u32x4 ffn_epi(const f32x16& acc) {
  u32x4 r;
  const f32x2 nL = {-1.4426950408889634f, -1.4426950408889634f}, one = {1.0f, 1.0f};
#pragma unroll
  for (int i = 0; i < 4; ++i) {
    const f32x2 g = {rbf(acc[2 * i]), rbf(acc[2 * i + 1])}, u = {rbf(acc[8 + 2 * i]), rbf(acc[9 + 2 * i])};
    const f32x2 t = g * nL;
    const f32x2 e = (f32x2){__builtin_amdgcn_exp2f(t[0]), __builtin_amdgcn_exp2f(t[1])} + one;
    const f32x2 sg = g * (f32x2){__builtin_amdgcn_rcpf(e[0]), __builtin_amdgcn_rcpf(e[1])};
    const f32x2 h = (f32x2){rbf(sg[0]), rbf(sg[1])} * u;
    r[i] = pk2(h[0], h[1]);
  }
  return r;
}

constexpr int L_S = 131072;
constexpr int L_PHI = L_S;
constexpr int L_V = L_S + 16384;
constexpr int L_CS = L_S + 24576;
constexpr int L_ST = L_S + 28672;
constexpr int L_ONES = L_S + 30720;

DEVI float dot2acc(unsigned a, unsigned b, float c) {
  return __builtin_amdgcn_fdot2_f32_bf16(__builtin_bit_cast(bf16x2, a), __builtin_bit_cast(bf16x2, b), c, false);
}
DEVI const char* uni(const void* p) {
  const unsigned long long v = (unsigned long long)p;
  const unsigned lo = __builtin_amdgcn_readfirstlane((unsigned)v), hi = __builtin_amdgcn_readfirstlane((unsigned)(v >> 32));
  return (const char*)(((unsigned long long)hi << 32) | lo);
}
#define ONE2 0x3f803f80u
#define RPRIO(n) __builtin_amdgcn_s_setprio(n)
#define SB0() __builtin_amdgcn_sched_barrier(0)
#define STMOD "sc0 sc1"
DEVI void gst(void* p, u32x4 v) { asm volatile("global_store_dwordx4 %0, %1, off " STMOD "\n\ts_nop 1" ::"v"(p), "v"(v) : "memory"); }
#define SGB(mask, n) __builtin_amdgcn_sched_group_barrier(mask, n, 0)

#define GATHER8(r, vo, sb0, sb1, O0, O1, O2, O3)                                                                   \
  asm volatile("global_load_ushort %0, %8, %9 offset:" #O0 "\n\tglobal_load_ushort %1, %8, %9 offset:" #O1          \
               "\n\tglobal_load_ushort %2, %8, %9 offset:" #O2 "\n\tglobal_load_ushort %3, %8, %9 offset:" #O3      \
               "\n\tglobal_load_ushort %4, %8, %10 offset:" #O0 "\n\tglobal_load_ushort %5, %8, %10 offset:" #O1    \
               "\n\tglobal_load_ushort %6, %8, %10 offset:" #O2 "\n\tglobal_load_ushort %7, %8, %10 offset:" #O3    \
               : "=&v"(r[0]), "=&v"(r[1]), "=&v"(r[2]), "=&v"(r[3]), "=&v"(r[4]), "=&v"(r[5]), "=&v"(r[6]), "=&v"(r[7]) \
               : "v"(vo), "s"(sb0), "s"(sb1)                                                                        \
               : "memory")
#define PIN8(r) asm volatile("" : "+v"(r[0]), "+v"(r[1]), "+v"(r[2]), "+v"(r[3]), "+v"(r[4]), "+v"(r[5]), "+v"(r[6]), "+v"(r[7]))

template <int N> struct IC { static constexpr int value = N; };
template <int... I, class F> DEVI void sfor_(std::integer_sequence<int, I...>, F& f) { (f(IC<I>{}), ...); }
template <int N, class F> DEVI void sfor(F f) { sfor_(std::make_integer_sequence<int, N>{}, f); }
DEVI void dma16(unsigned vo, const char* sb, unsigned dst) {
  asm volatile("s_mov_b32 m0, %2\n\ts_nop 0\n\tglobal_load_lds_dwordx4 %0, %1" ::"v"(vo), "s"(sb), "s"(dst) : "memory");
}

__global__ __launch_bounds__(512, 2) void k0_kernel(const u16* __restrict__ x, const u16* __restrict__ wqkv,
                                                    const u16* __restrict__ w1, const u16* __restrict__ w3,
                                                    char* __restrict__ ws) {
  __shared__ __attribute__((aligned(16))) char lds_raw[8 * 4096 + 8192];
  LAS char* lds = (LAS char*)lds_raw;
  const int b = blockIdx.x, tid = threadIdx.x, l = tid & 63;
  const int w = __builtin_amdgcn_readfirstlane(tid >> 6);
  ST(0, 12);
  const unsigned lw = (unsigned)(uintptr_t)lds + 4096 * w;
  u32x4 xr;
  asm volatile("global_load_dwordx4 %0, %1, off" : "=v"(xr) : "v"(x + (size_t)(b * 8 + w) * 512 + 8 * l) : "memory");
  SB0();
  const int it = w * 256 + b;
  const bool ffn = it < 704;
  const int j = ffn ? it : it - 704, ks = j & 31, g = j >> 5, u0 = min(4 * g, 82);
  {
    const size_t go = (size_t)(16 * ks * (ffn ? 2752 : 3072) + (ffn ? u0 * 32 : g * 256));
    const char* sa = uni((const char*)(ffn ? w1 : wqkv) + go);
    const char* sb = uni((const char*)(ffn ? w3 : wqkv + 8 * 1536) + go);
    const int st = ffn ? 8 * 2752 : 4 * 3072;
    const int sh = ffn ? 3 : 4;
    const unsigned vo = (unsigned)((l >> sh) * (ffn ? 2752 : 3072) + 16 * (l & ((1 << sh) - 1)));
    if (it < 1088) {
      dma16(vo, sa, lw);
      dma16(vo, sa + st, lw + 1024);
      dma16(vo, sb, lw + 2048);
      dma16(vo, sb + st, lw + 3072);
    }
  }
  const bool warm = w == 7 && b < 64;
  if (warm) {
    unsigned long long pc;
    asm volatile("s_getpc_b64 %0" : "=s"(pc));
    const char* cb = (const char*)(pc & ~127ull);
#pragma unroll
    for (int i = 0; i < 4; ++i) dma16((unsigned)l * 80u, cb + 5120 * i, lw + 1024 * i);
    WAIT_V(4);
  } else if (it < 1088) { WAIT_V(4); } else { WAIT_V(0); }
  asm volatile("" : "+v"(xr));
  float v[8];
#pragma unroll
  for (int i = 0; i < 4; ++i) { const unsigned t = xr[i]; v[2 * i] = lo16(t); v[2 * i + 1] = hi16(t); }
  const float mean = sum64(((v[0] + v[1]) + (v[2] + v[3])) + ((v[4] + v[5]) + (v[6] + v[7]))) * (1.0f / 512.0f);
  float sq = 0.f;
#pragma unroll
  for (int i = 0; i < 8; ++i) { const float d = v[i] - mean; sq += d * d; }
  sq = sum64(sq);
  const float mb = rbf(mean);
  const float sd = rbf(sqrtf(rbf(rbf(sq * (1.0f / 512.0f)) + 1e-5f)));
  const float rs = 1.0f / sd;
#pragma unroll
  for (int i = 0; i < 8; ++i) {
    const float d = rbf(v[i] - mb), q = d * rs;
    v[i] = __builtin_fmaf(__builtin_fmaf(-q, sd, d), rs, q);
  }
  u32x4 o;
#pragma unroll
  for (int i = 0; i < 4; ++i) o[i] = pk2(v[2 * i], v[2 * i + 1]);
  *(LAS u32x4*)(lds + 32768 + l * 128 + ((w ^ (l & 7)) * 16)) = o;
  WAIT_L0();
  BAR();
  ST(0, 13);
  {
    const int c16 = tid >> 3, r = tid & 7;
    const u32x4 t = *(const LAS u32x4*)(lds + 32768 + c16 * 128 + ((r ^ (c16 & 7)) * 16));
    gst(ws + WS_XN + (size_t)(b >> 2) * 32768 + c16 * 512 + ((b & 3) * 8 + r) * 16, t);
  }
  if (it < 1088) {
    WAIT_V(1);
    const int g4 = l >> 4, q4 = (l >> 2) & 3, p4 = l & 3;
    const int rd0 = ffn ? 2048 * (g4 & 1) + (8 * (g4 >> 1) + q4) * 128 + 16 * (p4 & 1) + 8 * (p4 >> 1)
                        : (8 * (g4 >> 1) + q4) * 256 + 32 * (g4 & 1) + 8 * p4;
    const int rstep = ffn ? 512 : 1024, fstep = ffn ? 32 : 64;
    const LAS char* p = lds + 4096 * w + rd0;
    char* d = ws + WS_WA + (size_t)((ffn ? 48 + u0 : 4 * g) * 32 + ks) * 1024 + 16 * l;
    u32x4 f[4];
#pragma unroll
    for (int i = 0; i < 4; ++i) f[i] = trfrag(p + fstep * i, rstep);
#pragma unroll
    for (int i = 0; i < 4; ++i) gst(d + 32768 * i, f[i]);
  }
  if (tid < 34) ((unsigned*)(ws + WS_KVR))[b * 34 + tid] = 0xffffffffu;
  if (warm) WAIT_V(0);
  ST(0, 14);
}

__global__ __launch_bounds__(512, 2) void k1_kernel(const u16* __restrict__ wphi, const u16* __restrict__ wo,
                                                    const u16* __restrict__ w2, char* __restrict__ ws) {
  __shared__ __attribute__((aligned(16))) char lds_raw[163840];
  LAS char* lds = (LAS char*)lds_raw;
  const int b = blockIdx.x, tid = threadIdx.x, l = tid & 63;
  const int w = __builtin_amdgcn_readfirstlane(tid >> 6);
  const int xcd = b & 7, idx = b >> 3;
  const int rb = (xcd & 1) * 8 + (idx & 7);
  const int c = (xcd >> 1) * 4 + (idx >> 3);
  const int nfb = c < 11 ? 3 : 2;
  const int fb0 = c < 11 ? 3 * c : 33 + 2 * (c - 11);
  const int rg = w & 3, hw = w >> 2;
  const int hh = l >> 5, l31 = l & 31;
  const int tg = rb * 4 + rg;
  ST(0, 0);

  const unsigned ldw = (unsigned)(uintptr_t)lds + 4096 * w;
  const unsigned vo = 16 * l;
  const char* sb_kv = uni(ws + WS_WA + (size_t)((16 * (1 + hw) + c) * 32 + 4 * rg) * 1024);
  const char* sb_q = uni(ws + WS_WA + (size_t)(c * 32 + 4 * w) * 1024);
  const char* sb_f = uni(ws + WS_WA + (size_t)((48 + 2 * fb0 + hw) * 32 + 4 * rg) * 1024);
  const char* sb_x = uni(ws + WS_XN + (size_t)((rb * 4 + (w >> 1)) * 32 + 8 * (w & 1)) * 1024);
  auto dma4 = [&](const char* sb, int slot) {
#pragma unroll
    for (int i = 0; i < 4; ++i) dma16(vo, sb + 1024 * i, ldw + slot + 1024 * i);
  };
  auto dma_kv = [&](int slot, int khalf) { dma4(sb_kv + khalf * 16384, slot); };
  auto dma_q = [&](int slot) { dma4(sb_q, slot); };
  auto dma_f = [&](int slot, int blk, int khalf) { dma4(sb_f + blk * 65536 + khalf * 16384, slot); };
  auto dma_x = [&](int khalf) {
#pragma unroll
    for (int i = 0; i < 8; ++i) dma16(vo, sb_x + khalf * 16384 + 1024 * i, ldw + 4096 * w + khalf * 65536 + 1024 * i);
  };
  dma_x(0);
  dma_kv(L_S, 0);
  BAR();
  dma_x(1);
  unsigned wg[8];
  {
    const unsigned go = (unsigned)(((l31 >> 4) * 256 + (l31 & 15) + 64 * hh) * 2);
    const char* sb = uni((const char*)wphi + c * 1024);
    GATHER8(wg, go, sb, sb + 256, 0, 32, 64, 96);
  }
  const int g4 = l >> 4, q4 = (l >> 2) & 3, p4 = l & 3;
  const int krow = 8 * (g4 >> 1) + q4;
  const int off_q = krow * 64 + 32 * (g4 & 1) + 8 * p4;
  const LAS char* pw = lds + 16384 * hw + 16 * l;
  auto wfrag = [&](const LAS char* p, int t) { return *(const LAS u32x4*)(p + 1024 * t); };
  const int tok = 32 * rg + l31;
  const LAS char* px = lds + 16384 * rg + 16 * l;

  WAIT_V(16);
  BAR();
  ST(0, 1);
  u32x4 xa[32];
#pragma unroll
  for (int t = 0; t < 16; ++t) xa[t] = wfrag(px, t);
  u32x4 bf[16];
#pragma unroll
  for (int t = 0; t < 4; ++t) bf[t] = wfrag(pw + L_S, t);
  WAIT_L0();
  BAR();
  ST(0, 2);

  f32x16 acc = {};
#pragma unroll
  for (int t = 0; t < 8; ++t) {
    acc = mfma32(bf[t], xa[t], acc);
    asm volatile("" : "+v"(acc));
    bf[t + 4] = wfrag(pw + L_S, t + 4);
    if (t < 4) dma16(vo, sb_kv + 16384 + 1024 * t, ldw + 1024 * t);
    else dma16(vo, sb_q + 1024 * (t - 4), ldw + 32768 + 1024 * (t - 4));
    SB0();
  }
  WAIT_V(8);
  BAR();
  PIN8(wg);
  u32x4 wp;
#pragma unroll
  for (int i = 0; i < 4; ++i) wp[i] = wg[2 * i] | (wg[2 * i + 1] << 16);
#pragma unroll
  for (int t = 8; t < 16; ++t) {
    acc = mfma32(bf[t], xa[t], acc);
    asm volatile("" : "+v"(acc));
    if (t + 4 < 16) bf[t + 4] = wfrag(pw + L_S, t + 4);
    xa[2 * t] = wfrag(px + 65536, 2 * t - 16);
    xa[2 * t + 1] = wfrag(px + 65536, 2 * t - 15);
    SB0();
  }
  WAIT_L0();
  WAIT_V(0);
  BAR();
  ST(0, 3);
  u32x4 fr[4];
  auto pre = [&](const LAS char* p) {
    SB0();
#pragma unroll
    for (int i = 0; i < 4; ++i) fr[i] = wfrag(p, i);
    SB0();
  };
  auto run = [&](f32x16& a, const LAS char* p, int x0, auto NC, auto DC, const char* src, unsigned dst) {
    constexpr int n = decltype(NC)::value, nd = decltype(DC)::value, ev = n / (nd ? nd : 1);
    RPRIO(2);
    SB0();
#pragma unroll
    for (int t = 0; t < n; ++t) {
      a = mfma32(fr[t & 3], xa[x0 + t], a);
      asm volatile("" : "+v"(a));
      if (t + 4 < n) fr[t & 3] = wfrag(p, t + 4);
      if (nd && t % ev == 0) dma16(vo, src + 1024 * (t / ev), dst + 1024 * (t / ev));
      SB0();
    }
    RPRIO(0);
  };
  const LAS char* pu = lds + 16 * l;
  const char* su = uni(ws + WS_WA + (size_t)((48 + 2 * fb0) * 32 + 8 * rg) * 1024);
  const unsigned du = (unsigned)(uintptr_t)lds + 8192 * rg;
  char* hdst = ws + WS_H + (size_t)(tg * NU + 2 * fb0) * 1024 + 16 * l;
  pre(pw);
  run(acc, pw, 16, IC<16>{}, IC<8>{}, su + (hw ? 0 : 32768), du + 65536 + 32768 * hw);
  f32x16 a2 = {};
  const int j2 = w * 80 + (c - 11) * 16 + rb, u2 = j2 >> 2, cb2 = j2 & 3;
  const bool cvt = c >= 11 && j2 < 472;
  auto cvt_issue = [&]() {
    if (cvt) {
      const char* sb = uni((u2 >= 86 ? (const char*)wo + (size_t)(u2 - 86) * 16384 : (const char*)w2 + (size_t)u2 * 16384) + cb2 * 256);
      const unsigned v2 = (unsigned)((l >> 4) * 1024 + 16 * (l & 15));
#pragma unroll
      for (int i = 0; i < 4; ++i) dma16(v2, sb + 4096 * i, ldw + 65536 + 1024 * i);
    }
  };
  unsigned ct0 = 0, ct1 = 0;
  const bool warm = w == 7 && b < 64;
  if (hw == 0) {
    if (tid < 8) {
      u32x4 ones = {ONE2, ONE2, ONE2, ONE2};
      *(LAS u32x4*)(lds + L_ONES + 16 * tid) = ones;
    }
    WAIT_L0();
    BAR();
    ST(0, 4);
    LAS char* pimg = lds + L_PHI + tok * 64 + hh * 16;
    const f32x16 p = fm_p(acc, wp, l31 >= 16);
    u32x4 ph[2];
    fm_head<0>(p, ph);
    *(LAS u32x4*)(pimg) = ph[0];
    *(LAS u32x4*)(pimg + 32) = ph[1];
    WAIT_V(0);
    BAR();
    ST(0, 5);
    fm_head<1>(p, ph);
    *(LAS u32x4*)(pimg + 8192) = ph[0];
    *(LAS u32x4*)(pimg + 8192 + 32) = ph[1];
    pre(pu + 98304);
    BAR();
    ST(0, 6);
    acc = (f32x16){};
    if (nfb == 3) run(acc, pu + 98304, 0, IC<32>{}, IC<8>{}, su + 4 * 32768, du + 65536);
    else { cvt_issue(); run(acc, pu + 98304, 0, IC<32>{}, IC<0>{}, su, du); }
    pre(pu);
    BAR();
    ST(0, 7);
    if (nfb == 3) { run(a2, pu, 0, IC<32>{}, IC<8>{}, su + 5 * 32768, du + 98304); WAIT_V(8); }
    else run(a2, pu, 0, IC<32>{}, IC<0>{}, su, du);
    BAR();
    ST(0, 8);
#pragma unroll 1
    for (int i = 0; i < 2; ++i) {
      gst(hdst + 2048 * i, ffn_epi(acc));
      acc = a2;
    }
    if (nfb == 3) {
      pre(pu + 65536);
      WAIT_V(2);
      BAR();
      ST(0, 9);
      acc = (f32x16){};
      run(acc, pu + 65536, 0, IC<32>{}, IC<0>{}, su, du);
      ST(0, 10);
      gst(hdst + 4096, ffn_epi(acc));
    }
    if (w < 2) {
      f32x16 kv = {};
      const LAS char* pa = lds + L_PHI + w * 8192 + off_q;
      const LAS char* pb = (g4 & 1) ? (lds + L_ONES + q4 * 32 + 8 * p4) : (lds + L_V + krow * 64 + 32 * w + 8 * p4);
      const int bstep = (g4 & 1) ? 0 : 1024, b4 = (g4 & 1) ? 0 : 256;
      u32x4 ka[8], kb[8];
#pragma unroll
      for (int t = 0; t < 8; ++t) { ka[t] = trfrag(pa + 1024 * t, 256); kb[t] = trfrag(pb + bstep * t, b4); }
      SB0();
#pragma unroll
      for (int t = 0; t < 8; ++t) kv = mfma32(ka[t], kb[t], kv);
      float* dst = (float*)(ws + WS_KVP) + (size_t)(rb * 32 + 2 * c + w) * 544;
      if (l31 <= 16) {
#pragma unroll
        for (int a = 0; a < 4; ++a) {
          f32x4 o = {kv[4 * a], kv[4 * a + 1], kv[4 * a + 2], kv[4 * a + 3]};
          const int e = l31 < 16 ? (a * 16 + l31) * 8 + 4 * hh : 512 + 8 * a + 4 * hh;
          gst(dst + e, __builtin_bit_cast(u32x4, o));
        }
      }
    }
  } else {
    pre(pu + 32768);
    WAIT_L0();
    BAR();
    ST(0, 4);
    run(a2, pu + 32768, 0, IC<32>{}, IC<8>{}, su + 2 * 32768, du);
#pragma unroll
    for (int a = 0; a < 4; ++a) {
      u32x2 o = {pk2(acc[4 * a], acc[4 * a + 1]), pk2(acc[4 * a + 2], acc[4 * a + 3])};
      *(LAS u32x2*)(lds + L_V + tok * 64 + (8 * a + 4 * hh) * 2) = o;
    }
    WAIT_V(8);
    BAR();
    ST(0, 5);
    acc = (f32x16){};
    pre(pu + 65536);
    run(acc, pu + 65536, 0, IC<32>{}, IC<8>{}, su + 3 * 32768, du + 32768);
    WAIT_V(8);
    BAR();
    ST(0, 6);
    char* dst = ws + WS_PQ + (size_t)(tg * 32 + 2 * c) * 2048 + 16 * l;
    const f32x16 p = fm_p(a2, wp, l31 >= 16);
    u32x4 ph[2];
    fm_head<0>(p, ph);
    gst(dst, ph[0]);
    gst(dst + 1024, ph[1]);
    WAIT_V(2);
    BAR();
    ST(0, 7);
    cvt_issue();
    if (warm) {
      unsigned long long pc;
      asm volatile("s_getpc_b64 %0" : "=s"(pc));
      asm volatile("global_load_dword %0, %2, %3\n\tglobal_load_dword %1, %2, %4"
                   : "=&v"(ct0), "=&v"(ct1) : "v"(128 * l), "s"((pc & ~127ull) + 8192), "s"((pc & ~127ull) + 16384) : "memory");
    }
    fm_head<1>(p, ph);
    gst(dst + 2048, ph[0]);
    gst(dst + 3072, ph[1]);
    pre(pu + 32768);
    BAR();
    ST(0, 8);
    a2 = (f32x16){};
    run(a2, pu + 32768, 0, IC<32>{}, IC<0>{}, su, du);
    if (nfb == 3) {
      BAR();
      ST(0, 9);
    }
#pragma unroll 1
    for (int i = 0; i < 2; ++i) {
      gst(hdst + 1024 + 2048 * i, ffn_epi(acc));
      acc = a2;
    }
    if (nfb == 3) {
      ST(0, 10);
      acc = (f32x16){};
      pre(pu + 98304);
      run(acc, pu + 98304, 0, IC<32>{}, IC<0>{}, su, du);
      gst(hdst + 5120, ffn_epi(acc));
    }
  }
  if (cvt) {
    const bool perm = u2 >= 86;
    WAIT_V(0);
    const LAS char* p = lds + 65536 + 4096 * w + ((perm ? 4 : 8) * (g4 >> 1) + q4) * 256 + 32 * (g4 & 1) + 8 * p4;
    char* d = ws + WS_W2 + (size_t)(2 * cb2 * 118 + u2) * 2048 + 16 * l;
    u32x4 f[4];
#pragma unroll
    for (int i = 0; i < 4; ++i) f[i] = trfrag(p + 64 * i, perm ? 2048 : 1024);
#pragma unroll
    for (int i = 0; i < 4; ++i) gst(d + 1024 * i + (118 * 2048 - 2048) * (i >> 1), f[i]);
  }
  if (warm) { WAIT_V(0); asm volatile("" ::"v"(ct0), "v"(ct1)); }
  ST(0, 11);
}

#define FPD 4
#define KWAIT 0
#define PRIO(n) __builtin_amdgcn_s_setprio(n)
constexpr int RED_PITCH = 272;

__global__ __launch_bounds__(512, 2) void k2_kernel(const u16* __restrict__ x, char* ws, u16* __restrict__ out) {
  __shared__ __attribute__((aligned(16))) char lds_raw[8 * 64 * RED_PITCH];
  LAS char* lds = (LAS char*)lds_raw;
  const int b = blockIdx.x, tid = threadIdx.x, l = tid & 63;
  const int w = __builtin_amdgcn_readfirstlane(tid >> 6);
  const int xcd = b & 7, idx = b >> 3;
  const int rb = xcd * 4 + (idx & 3);
  const int ct = idx >> 2;
  const int hh = l >> 5, l31 = l & 31;
  const int tg0 = 2 * rb, nt0 = 2 * ct;
  const bool prod = (w == 0) && (b < 136);
  ST(1, 0);
  f32x4 pt[8];
  if (prod) {
    const f32x4* p = (const f32x4*)(ws + WS_KVP) + (size_t)hh * 8 * 4352 + b * 32 + l31;
#pragma unroll
    for (int r = 0; r < 8; ++r) pt[r] = p[(size_t)r * 4352];
  }
  SB0();
  BAR();
  SB0();
  const size_t xoff = (size_t)(rb * 64 + (tid >> 3)) * 512 + ct * 64 + 8 * (tid & 7);
  u32x4 xv;
  f32x16 acc[2][2] = {};
  if (w >= 4) {
    const int u0 = w < 6 ? 22 * (w - 4) : 44 + 21 * (w - 6);
    const bool full = w < 6;
    const char* pa = ws + WS_H + (size_t)(tg0 * NU + u0) * 1024 + 16 * l;
    const char* pb = ws + WS_W2 + (size_t)(ct * 118 + u0) * 2048 + 16 * l;
    constexpr int PD = FPD;
    u32x4 fa[PD][2], fb[PD][2];
#pragma unroll
    for (int s = 0; s < PD; ++s) {
      fa[s][0] = *(const u32x4*)(pa + s * 1024);
      fa[s][1] = *(const u32x4*)(pa + s * 1024 + NU * 1024);
      fb[s][0] = *(const u32x4*)(pb + s * 2048);
      fb[s][1] = *(const u32x4*)(pb + s * 2048 + 1024);
    }
    SB0();
    sfor<22>([&](auto SC) {
      constexpr int s = decltype(SC)::value, r = s % PD;
      if (s < 21 || full) {
        acc[0][0] = mfma32(fb[r][0], fa[r][0], acc[0][0]);
        acc[0][1] = mfma32(fb[r][0], fa[r][1], acc[0][1]);
        acc[1][0] = mfma32(fb[r][1], fa[r][0], acc[1][0]);
        acc[1][1] = mfma32(fb[r][1], fa[r][1], acc[1][1]);
      }
      SB0();
      if (s + PD < 21 || (s + PD == 21 && full)) {
        fa[r][0] = *(const u32x4*)(pa + (s + PD) * 1024);
        fa[r][1] = *(const u32x4*)(pa + (s + PD) * 1024 + NU * 1024);
        fb[r][0] = *(const u32x4*)(pb + (s + PD) * 2048);
        fb[r][1] = *(const u32x4*)(pb + (s + PD) * 2048 + 1024);
      }
      if (s + PD == 22) xv = *(const u32x4*)(x + xoff);
      SB0();
    });
  } else {
    PRIO(2);
    const char* pq = ws + WS_PQ + (size_t)(tg0 * 32 + 8 * w) * 2048 + 16 * l;
    const char* pw = ws + WS_W2 + (size_t)(ct * 118 + 86 + 8 * w) * 2048 + 16 * l;
    const unsigned kdst = (unsigned)(uintptr_t)lds + w * (64 * RED_PITCH);
    const char* ksrc = uni(ws + WS_KVR + w * 8704);
    const LAS char* pk = lds + w * (64 * RED_PITCH) + (l31 < 16 ? (hh * 16 + l31) * 16 : 1024 + hh * 16);
    const int kstep = l31 < 16 ? 512 : 32;
    auto dmaK = [&]() {
      const unsigned vo = 16 * l;
#pragma unroll
      for (int i = 0; i < 8; ++i)
        asm volatile("s_mov_b32 m0, %2\n\ts_nop 0\n\tglobal_load_lds_dwordx4 %0, %1 sc1" ::"v"(vo), "s"(ksrc + 1024 * i), "s"(kdst + 1024 * i) : "memory");
      if (l < 32)
        asm volatile("s_mov_b32 m0, %2\n\ts_nop 0\n\tglobal_load_lds_dwordx4 %0, %1 sc1" ::"v"(vo), "s"(ksrc + 8192), "s"(kdst + 8192) : "memory");
    };
    u32x4 fq[8][4], fw[8][2], fk[8][2];
    f32x16 n[16];
    auto ldQ = [&](auto SC) {
      constexpr int s = decltype(SC)::value;
      fq[s][0] = *(const u32x4*)(pq + s * 2048);
      fq[s][1] = *(const u32x4*)(pq + s * 2048 + 1024);
      fq[s][2] = *(const u32x4*)(pq + s * 2048 + 65536);
      fq[s][3] = *(const u32x4*)(pq + s * 2048 + 65536 + 1024);
      fw[s][0] = *(const u32x4*)(pw + s * 2048);
      fw[s][1] = *(const u32x4*)(pw + s * 2048 + 1024);
      SB0();
    };
    auto rdK = [&](auto SC) {
      constexpr int s = decltype(SC)::value;
      fk[s][0] = *(const LAS u32x4*)(pk + s * 1088);
      fk[s][1] = *(const LAS u32x4*)(pk + s * 1088 + kstep);
    };
    auto num = [&](auto UC) {
      constexpr int u = decltype(UC)::value, s = u >> 1, g = u & 1;
      if (g == 0) {
        for (int spin = 0; spin < (1 << 16); ++spin) {
          const unsigned m0 = max(max(fk[s][0][0], fk[s][0][1]), max(fk[s][0][2], fk[s][0][3]));
          const unsigned m1 = max(max(fk[s][1][0], fk[s][1][1]), max(fk[s][1][2], fk[s][1][3]));
          if (__builtin_amdgcn_ballot_w64(max(m0, m1) == 0xffffffffu) == 0) break;
          __builtin_amdgcn_s_sleep(4);
          dmaK();
          WAIT_V(0);
          rdK(IC<s>{});
        }
        if (s < 7) rdK(IC<(s + 1) & 7>{});
      }
      const f32x16 z = {};
      n[u] = mfma32(fk[s][0], fq[s][2 * g], z);
      n[u] = mfma32(fk[s][1], fq[s][2 * g + 1], n[u]);
      SB0();
    };
    auto proj = [&](auto UC) {
      constexpr int u = decltype(UC)::value, s = u >> 1, g = u & 1;
      u32x4 of;
      const float rd = __builtin_amdgcn_rcpf(rbf(rbf(n[u][8]) + 1e-6f));
#pragma unroll
      for (int i = 0; i < 4; ++i) of[i] = pk2(rbf(n[u][2 * i]) * rd, rbf(n[u][2 * i + 1]) * rd);
      acc[0][g] = mfma32(fw[s][0], of, acc[0][g]);
      acc[1][g] = mfma32(fw[s][1], of, acc[1][g]);
      SB0();
    };
    ldQ(IC<0>{}); ldQ(IC<1>{});
    if (prod) {
      f32x4 sacc = pt[0];
#pragma unroll
      for (int r = 1; r < 8; ++r) sacc += pt[r];
      float s4[4];
#pragma unroll
      for (int e = 0; e < 4; ++e) { const float v = sacc[e]; s4[e] = xhalf_sum(v); }
      const unsigned long long v = (unsigned long long)pk2(s4[0], s4[1]) | ((unsigned long long)pk2(s4[2], s4[3]) << 32);
      if (l < 32) __hip_atomic_store((GAS unsigned long long*)(ws + WS_KVR) + b * 32 + l31, v, __ATOMIC_RELAXED, __HIP_MEMORY_SCOPE_AGENT);
      ST(1, 1);
    }
    SB0();
    WAIT_V(7); SB0();
    ldQ(IC<2>{});
    WAIT_V(7); SB0();
    ldQ(IC<3>{});
    WAIT_V(KWAIT); SB0();
    ST(1, 2);
    dmaK();
    ldQ(IC<4>{});
    WAIT_V(6); SB0();
    rdK(IC<0>{});
    num(IC<0>{});
    ST(1, 6);
    sfor<16>([&](auto UC) {
      constexpr int u = decltype(UC)::value;
      if (u < 15) num(IC<(u + 1) & 15>{});
      proj(UC);
      if ((u & 1) && (u >> 1) + 5 < 8) ldQ(IC<((u >> 1) + 5) & 7>{});
      if (u == 7) { xv = *(const u32x4*)(x + xoff); SB0(); }
    });
    WAIT_L0();
  }
  ST(1, 3);
  {
    LAS char* dst = lds + w * (64 * RED_PITCH) + l31 * RED_PITCH + 16 * hh;
#pragma unroll
    for (int nn = 0; nn < 2; ++nn)
#pragma unroll
      for (int g = 0; g < 2; ++g)
#pragma unroll
        for (int a = 0; a < 4; ++a) {
          f32x4 o = {acc[nn][g][4 * a], acc[nn][g][4 * a + 1], acc[nn][g][4 * a + 2], acc[nn][g][4 * a + 3]};
          *(LAS f32x4*)(dst + g * 32 * RED_PITCH + nn * 128 + a * 32) = o;
        }
  }
  WAIT_L0();
  BAR();
  ST(1, 4);
  {
    const LAS char* src = lds + (tid >> 3) * RED_PITCH + 32 * (tid & 7);
    f32x4 f0 = {}, f1 = {}, s0 = {}, s1 = {};
#pragma unroll
    for (int p = 0; p < 4; ++p) {
      s0 += *(const LAS f32x4*)(src + p * 64 * RED_PITCH);
      s1 += *(const LAS f32x4*)(src + p * 64 * RED_PITCH + 16);
      f0 += *(const LAS f32x4*)(src + (p + 4) * 64 * RED_PITCH);
      f1 += *(const LAS f32x4*)(src + (p + 4) * 64 * RED_PITCH + 16);
    }
    float y[8];
#pragma unroll
    for (int j = 0; j < 8; ++j) {
      const float f = rbf(j < 4 ? f0[j & 3] : f1[j & 3]);
      const float sv = rbf(j < 4 ? s0[j & 3] : s1[j & 3]);
      const float g = rbf(silu_f(f));
      const float xx = (j & 1) ? hi16(xv[j >> 1]) : lo16(xv[j >> 1]);
      y[j] = xx + rbf(sv * g);
    }
    u32x4 o;
#pragma unroll
    for (int i = 0; i < 4; ++i) o[i] = pk2(y[2 * i], y[2 * i + 1]);
    *(u32x4*)(out + xoff) = o;
  }
  ST(1, 5);
}

#if defined(__HIP_DEVICE_COMPILE__)
#pragma clang attribute pop
#endif

extern "C" void kernel_launch(void* const* d_in, const int* in_sizes, int n_in, void* d_out, int out_size, void* d_ws,
                              size_t ws_size, hipStream_t stream) {
  const u16* x = (const u16*)d_in[0];
  const u16* wqkv = (const u16*)d_in[1];
  const u16* wphi = (const u16*)d_in[2];
  const u16* wo = (const u16*)d_in[3];
  const u16* w1 = (const u16*)d_in[5];
  const u16* w3 = (const u16*)d_in[6];
  const u16* w2 = (const u16*)d_in[7];
  char* ws = (char*)d_ws;
  k0_kernel<<<256, 512, 0, stream>>>(x, wqkv, w1, w3, ws);
  k1_kernel<<<256, 512, 0, stream>>>(wphi, wo, w2, ws);
  k2_kernel<<<256, 512, 0, stream>>>(x, ws, (u16*)d_out);
}
```
